```python
import torch

def _softmax(x, axis=-1):
    return torch.softmax(x, dim=axis)
def _gelu(x):
    return torch.nn.functional.gelu(x, approximate="tanh")
def _silu(x):
    return torch.nn.functional.silu(x)
def _sigmoid(x):
    return torch.sigmoid(x)
def _layernorm(x, axis=-1, eps=1e-5):
    m = x.mean(dim=axis, keepdim=True)
    v = x.var(dim=axis, keepdim=True, unbiased=False)
    return (x - m) / torch.sqrt(v + eps)
def _scatter_add(tgt, idx, src, axis=0):
    return tgt.clone().index_add_(axis, idx, src)
def _segment_sum(src, seg, n):
    out = torch.zeros((n,) + src.shape[1:], dtype=src.dtype)
    return out.index_add_(0, seg, src)
def _pad(x, pw):
    flat = tuple(v for pair in reversed(pw) for v in pair)
    return torch.nn.functional.pad(x, flat)

kernel_name = 'concat_heads__gqa_block_cached__gqa_block_cached__near_ridge'

def setup_inputs():

    _w = (lambda a: (lambda t: t.to(torch.bfloat16) if t.is_floating_point() else t)(torch.as_tensor(a)))
    def _A_setup():
        B, Sq, D = 8, 64, 448
        Hq, Hkv, Sk, Dh = 16, 8, 1024, 256
        assert Hq % Hkv == 0, (Hq, Hkv)
        return {
            "x": _w((lambda *s: torch.randn(*s, dtype=torch.bfloat16) * 0.8)(B, Sq, D)),
            "k_cache": _w((lambda *s: torch.randn(*s, dtype=torch.bfloat16) * 0.8)(B, Hkv, Sk, Dh)),
            "v_cache": _w((lambda *s: torch.randn(*s, dtype=torch.bfloat16) * 0.05)(B, Hkv, Sk, Dh)),
            "wq": _w((lambda *s: torch.randn(*s, dtype=torch.bfloat16) * 0.8)(D, Hq * Dh) * (D ** -0.5)),
            "wo": _w((lambda *s: torch.randn(*s, dtype=torch.bfloat16) * 0.05)(Hq * Dh, D) * ((Hq * Dh) ** -0.5)),
        }
    _A_in = _A_setup()

    def _B_setup():
        B, Sq, D = 8, 64, 448
        Hq, Hkv, Sk, Dh = 16, 8, 1024, 256
        assert Hq % Hkv == 0, (Hq, Hkv)
        return {
            "x": _w((lambda *s: torch.randn(*s, dtype=torch.bfloat16) * 0.8)(B, Sq, D)),
            "k_cache": _w((lambda *s: torch.randn(*s, dtype=torch.bfloat16) * 0.8)(B, Hkv, Sk, Dh)),
            "v_cache": _w((lambda *s: torch.randn(*s, dtype=torch.bfloat16) * 0.05)(B, Hkv, Sk, Dh)),
            "wq": _w((lambda *s: torch.randn(*s, dtype=torch.bfloat16) * 0.8)(D, Hq * Dh) * (D ** -0.5)),
            "wo": _w((lambda *s: torch.randn(*s, dtype=torch.bfloat16) * 0.05)(Hq * Dh, D) * ((Hq * Dh) ** -0.5)),
        }
    _B_in = _B_setup()

    return {
        "A_x": _A_in["x"],
        "A_k_cache": _A_in["k_cache"],
        "A_v_cache": _A_in["v_cache"],
        "A_wq": _A_in["wq"],
        "A_wo": _A_in["wo"],
        "B_x": _B_in["x"],
        "B_k_cache": _B_in["k_cache"],
        "B_v_cache": _B_in["v_cache"],
        "B_wq": _B_in["wq"],
        "B_wo": _B_in["wo"],
    }

def reference(A_x, A_k_cache, A_v_cache, A_wq, A_wo, B_x, B_k_cache, B_v_cache, B_wq, B_wo):
    def _A_ref(x, k_cache, v_cache, wq, wo):
        B, Sq, D = x.shape
        Hq, Dh = 16, 256
        Hkv = k_cache.shape[1]
        rep = Hq // Hkv
        q = (lambda x, *d: x.permute(*d))((x @ wq).reshape(B, Sq, Hq, Dh), 0, 2, 1, 3)
        k = (lambda x, n, axis: torch.repeat_interleave(x, n, dim=axis))(k_cache, rep, axis=1)
        v = (lambda x, n, axis: torch.repeat_interleave(x, n, dim=axis))(v_cache, rep, axis=1)
        scores = torch.einsum("bhqd,bhkd->bhqk", q, k) * (Dh ** -0.5)
        p = _softmax(scores, axis=-1)
        o = torch.einsum("bhqk,bhkd->bhqd", p, v)
        return (lambda x, *d: x.permute(*d))(o, 0, 2, 1, 3).reshape(B, Sq, Hq * Dh) @ wo

    def _B_ref(x, k_cache, v_cache, wq, wo):
        B, Sq, D = x.shape
        Hq, Dh = 16, 256
        Hkv = k_cache.shape[1]
        rep = Hq // Hkv
        q = (lambda x, *d: x.permute(*d))((x @ wq).reshape(B, Sq, Hq, Dh), 0, 2, 1, 3)
        k = (lambda x, n, axis: torch.repeat_interleave(x, n, dim=axis))(k_cache, rep, axis=1)
        v = (lambda x, n, axis: torch.repeat_interleave(x, n, dim=axis))(v_cache, rep, axis=1)
        scores = torch.einsum("bhqd,bhkd->bhqk", q, k) * (Dh ** -0.5)
        p = _softmax(scores, axis=-1)
        o = torch.einsum("bhqk,bhkd->bhqd", p, v)
        return (lambda x, *d: x.permute(*d))(o, 0, 2, 1, 3).reshape(B, Sq, Hq * Dh) @ wo

    a = _A_ref(A_x, A_k_cache, A_v_cache, A_wq, A_wo)
    b = _B_ref(B_x, B_k_cache, B_v_cache, B_wq, B_wo)
    return torch.concatenate([a, b], axis=-1)
```

```cpp
#include <hip/hip_runtime.h>
#include <hip/hip_bf16.h>
typedef unsigned short u16;
__device__ __forceinline__ float bf2f(u16 x){ return __uint_as_float(((unsigned)x)<<16); }
__device__ __forceinline__ u16 f2bf(float f){ unsigned u=__float_as_uint(f); u += 0x7fff + ((u>>16)&1); return (u16)(u>>16); }
constexpr int B=8,SQ=64,D=448,HQ=16,HKV=8,SK=1024,DH=256;
__global__ void qproj(const u16* x, const u16* wq, u16* q){
  int row = blockIdx.x;
  __shared__ float xs[D];
  for(int i=threadIdx.x;i<D;i+=256) xs[i]=bf2f(x[row*D+i]);
  __syncthreads();
  for(int c=threadIdx.x;c<HQ*DH;c+=256){
    float a=0; for(int k=0;k<D;k++) a+=xs[k]*bf2f(wq[(size_t)k*HQ*DH+c]);
    q[(size_t)row*HQ*DH+c]=f2bf(a);
  }
}
__global__ void attn(const u16* q, const u16* kc, const u16* vc, u16* o){
  int row=blockIdx.x, h=blockIdx.y; int b=row/SQ; int g=h/2;
  __shared__ float qs[DH]; __shared__ float s[SK]; __shared__ float red[256];
  int t=threadIdx.x;
  qs[t]=bf2f(q[(size_t)row*HQ*DH+h*DH+t]);
  __syncthreads();
  const u16* K=kc+((size_t)(b*HKV+g))*SK*DH; const u16* V=vc+((size_t)(b*HKV+g))*SK*DH;
  float mx=-1e30f;
  for(int k=t;k<SK;k+=256){ float a=0; for(int d=0;d<DH;d++) a+=qs[d]*bf2f(K[(size_t)k*DH+d]); a=bf2f(f2bf(a))*0.0625f; s[k]=a; mx=fmaxf(mx,a);}
  red[t]=mx; __syncthreads();
  for(int st=128;st>0;st>>=1){ if(t<st) red[t]=fmaxf(red[t],red[t+st]); __syncthreads(); }
  mx=red[0]; __syncthreads();
  float sum=0; for(int k=t;k<SK;k+=256){ float e=__expf(s[k]-mx); s[k]=e; sum+=e; }
  red[t]=sum; __syncthreads();
  for(int st=128;st>0;st>>=1){ if(t<st) red[t]+=red[t+st]; __syncthreads(); }
  sum=red[0];
  float a=0; for(int k=0;k<SK;k++) a+=bf2f(f2bf(s[k]/sum))*bf2f(V[(size_t)k*DH+t]);
  o[(size_t)row*HQ*DH+h*DH+t]=f2bf(a);
}
__global__ void oproj(const u16* o, const u16* wo, u16* out, int coff){
  int row=blockIdx.x; __shared__ float os[HQ*DH];
  for(int i=threadIdx.x;i<HQ*DH;i+=256) os[i]=bf2f(o[(size_t)row*HQ*DH+i]);
  __syncthreads();
  for(int c=threadIdx.x;c<D;c+=256){ float a=0; for(int k=0;k<HQ*DH;k++) a+=os[k]*bf2f(wo[(size_t)k*D+c]); out[(size_t)row*2*D+coff+c]=f2bf(a);}
}
extern "C" void kernel_launch(void* const* d_in, const int* in_sizes, int n_in, void* d_out, int out_size, void* d_ws, size_t ws_size, hipStream_t stream){
  u16* q=(u16*)d_ws; u16* o=q+(size_t)512*4096;
  for(int p=0;p<2;p++){
    const u16* x=(const u16*)d_in[p*5+0]; const u16* k=(const u16*)d_in[p*5+1]; const u16* v=(const u16*)d_in[p*5+2];
    const u16* wq=(const u16*)d_in[p*5+3]; const u16* wo=(const u16*)d_in[p*5+4];
    qproj<<<512,256,0,stream>>>(x,wq,q);
    attn<<<dim3(512,16),256,0,stream>>>(q,k,v,o);
    oproj<<<512,256,0,stream>>>(o,wo,(u16*)d_out,p*D);
  }
}
```

```cpp
#include <hip/hip_runtime.h>

__global__ __launch_bounds__(256, 1) void gqa_fused(const char* xA, const char* kA, const char* vA, const char* wqA, const char* woA,
                                                    const char* xB, const char* kB, const char* vB, const char* wqB, const char* woB,
                                                    char* outp, char* ws) {
  unsigned tid = threadIdx.x;
  asm volatile(
    "v_mov_b32 v1, 0xff800000\n\t"
    "v_accvgpr_write_b32 a254, v1\n\t"
    "v_accvgpr_write_b32 a255, v1\n\t"
    "s_mov_b32 s79, 0\n\t"
    "Lstart_%=:\n\t"
    "s_lshr_b32 s73, %[wg], 2\n\t"
    "s_and_b32 s73, s73, 1\n\t"
    "s_mul_i32 s74, s73, 40\n\t"
    "s_load_dwordx8 s[4:11], %[ka], s74\n\t"
    "s_load_dwordx2 s[12:13], %[ka], s74 offset:32\n\t"
    "s_load_dwordx4 s[16:19], %[ka], 0x50\n\t"
    "v_readfirstlane_b32 s70, v0\n\t"
    "v_and_b32 v223, 63, v0\n\t"
    "v_lshrrev_b32 v219, 3, v223\n\t"
    "v_and_b32 v208, 7, v223\n\t"
    "v_lshrrev_b32 v209, 4, v223\n\t"
    "v_xor_b32 v221, v208, v209\n\t"
    "v_lshlrev_b32 v221, 4, v221\n\t"
    "v_and_b32 v209, 1, v209\n\t"
    "v_lshlrev_b32 v210, 2, v209\n\t"
    "v_xor_b32 v222, v208, v210\n\t"
    "v_lshlrev_b32 v222, 4, v222\n\t"
    "v_mul_u32_u24 v211, 896, v219\n\t"
    "v_add_u32 v211, v211, v221\n\t"
    "v_lshl_add_u32 v194, v219, 13, v222\n\t"
    "v_xor_b32 v195, 32, v194\n\t"
    "v_add_u32 v195, 64512, v195\n\t"
    "v_add_u32 v196, 129024, v194\n\t"
    "v_add_u32 v197, 129024, v195\n\t"
    "s_lshr_b32 s72, %[wg], 5\n\t"
    "s_and_b32 s74, %[wg], 3\n\t"
    "s_lshr_b32 s75, %[wg], 3\n\t"
    "s_and_b32 s75, s75, 3\n\t"
    "s_lshl2_add_u32 s71, s74, s75\n\t"
    "s_lshr_b32 s70, s70, 6\n\t"
    "s_mul_i32 s56, s70, 40960\n\t"
    "s_and_b32 s76, s71, 1\n\t"
    "s_lshl_b32 s77, s76, 6\n\t"
    "v_xor_b32 v211, s77, v211\n\t"
    "s_mul_i32 s77, s76, 7168\n\t"
    "v_add_u32 v192, s77, v211\n\t"
    "s_lshl_b32 s76, s76, 10\n\t"
    "s_add_u32 s65, s56, s76\n\t"
    "s_xor_b32 s66, s65, 1024\n\t"
    "s_sub_u32 s67, 7168, s77\n\t"
    "s_cmp_eq_u32 s72, 7\n\t"
    "s_cselect_b32 s57, 0, s72\n\t"
    "s_lshl_b32 s57, s57, 19\n\t"
    "s_add_u32 s58, s57, 0x80000\n\t"
    "s_sub_u32 s76, s58, 0x380000\n\t"
    "s_cselect_b32 s58, s58, s76\n\t"
    "s_add_u32 s59, s58, 0x80000\n\t"
    "s_sub_u32 s76, s59, 0x380000\n\t"
    "s_cselect_b32 s59, s59, s76\n\t"
    "s_add_u32 s60, s59, 0x80000\n\t"
    "s_sub_u32 s76, s60, 0x380000\n\t"
    "s_cselect_b32 s60, s60, s76\n\t"
    "s_add_u32 s61, s60, 0x80000\n\t"
    "s_sub_u32 s76, s61, 0x380000\n\t"
    "s_cselect_b32 s61, s61, s76\n\t"
    "s_add_u32 s62, s61, 0x80000\n\t"
    "s_sub_u32 s76, s62, 0x380000\n\t"
    "s_cselect_b32 s62, s62, s76\n\t"
    "s_add_u32 s63, s62, 0x80000\n\t"
    "s_sub_u32 s76, s63, 0x380000\n\t"
    "s_cselect_b32 s63, s63, s76\n\t"
    "s_lshl2_add_u32 s74, s72, s70\n\t"
    "s_mul_i32 s74, s74, 14336\n\t"
    "s_lshl2_add_u32 s75, s71, s70\n\t"
    "s_lshl_b32 s75, s75, 7\n\t"
    "s_sub_u32 s77, 3670016, s75\n\t"
    "s_mov_b32 s22, 14336\n\t"
    "s_mov_b32 s23, 0x20000\n\t"
    "s_mov_b32 s26, s77\n\t"
    "s_mov_b32 s27, 0x20000\n\t"
    "s_waitcnt lgkmcnt(0)\n\t"
    "s_add_u32 s20, s4, s74\n\t"
    "s_addc_u32 s21, s5, 0\n\t"
    "s_and_b32 s21, s21, 0xffff\n\t"
    "s_add_u32 s24, s10, s75\n\t"
    "s_addc_u32 s25, s11, 0\n\t"
    "s_and_b32 s25, s25, 0xffff\n\t"
    "s_add_u32 m0, s65, 26624\n\t"
    "s_lshr_b32 s80, s57, 12\n\t"
    "buffer_load_dwordx4 v192, s[20:23], s80 offen lds\n\t"
    "s_add_u32 m0, s65, 28672\n\t"
    "s_lshr_b32 s80, s58, 12\n\t"
    "buffer_load_dwordx4 v192, s[20:23], s80 offen lds\n\t"
    "s_add_u32 m0, s65, 30720\n\t"
    "s_lshr_b32 s80, s59, 12\n\t"
    "buffer_load_dwordx4 v192, s[20:23], s80 offen lds\n\t"
    "s_add_u32 m0, s65, 32768\n\t"
    "s_lshr_b32 s80, s60, 12\n\t"
    "buffer_load_dwordx4 v192, s[20:23], s80 offen lds\n\t"
    "s_add_u32 m0, s65, 34816\n\t"
    "s_lshr_b32 s80, s61, 12\n\t"
    "buffer_load_dwordx4 v192, s[20:23], s80 offen lds\n\t"
    "s_add_u32 m0, s65, 36864\n\t"
    "s_lshr_b32 s80, s62, 12\n\t"
    "buffer_load_dwordx4 v192, s[20:23], s80 offen lds\n\t"
    "s_add_u32 m0, s65, 38912\n\t"
    "s_lshr_b32 s80, s63, 12\n\t"
    "buffer_load_dwordx4 v192, s[20:23], s80 offen lds\n\t"
    "s_add_u32 m0, s56, 0\n\t"
    "s_add_u32 s80, s57, 0\n\t"
    "buffer_load_dwordx4 v194, s[24:27], s80 offen lds\n\t"
    "buffer_load_dwordx4 v195, s[24:27], s80 offen offset:1024 lds\n\t"
    "buffer_load_dwordx4 v196, s[24:27], s80 offen offset:2048 lds\n\t"
    "buffer_load_dwordx4 v197, s[24:27], s80 offen offset:3072 lds\n\t"
    "v_xor_b32 v211, 64, v211\n\t"
    "v_add_u32 v193, s67, v211\n\t"
    "v_lshrrev_b32 v208, 5, v223\n\t"
    "v_bfe_u32 v210, v223, 2, 2\n\t"
    "v_lshrrev_b32 v211, 1, v210\n\t"
    "v_lshlrev_b32 v211, 6, v211\n\t"
    "v_lshl_add_u32 v220, v209, 5, v211\n\t"
    "v_lshl_add_u32 v220, v210, 7, v220\n\t"
    "v_lshrrev_b32 v207, 4, v223\n\t"
    "v_and_b32 v211, 3, v223\n\t"
    "v_lshl_add_u32 v211, v211, 3, v220\n\t"
    "v_lshl_add_u32 v211, v207, 10, v211\n\t"
    "v_add_u32 v198, s56, v211\n\t"
    "v_xor_b32 v199, 32, v198\n\t"
    "v_xor_b32 v200, 64, v198\n\t"
    "v_xor_b32 v201, 96, v198\n\t"
    "v_bfe_u32 v211, v223, 1, 3\n\t"
    "v_xor_b32 v212, v211, v208\n\t"
    "v_lshlrev_b32 v214, 4, v212\n\t"
    "v_xor_b32 v211, v211, v207\n\t"
    "v_lshlrev_b32 v211, 4, v211\n\t"
    "v_and_b32 v212, 15, v223\n\t"
    "v_lshl_add_u32 v211, v212, 7, v211\n\t"
    "v_add_u32 v202, 26624, v211\n\t"
    "v_add_u32 v204, 108544, v211\n\t"
    "v_xor_b32 v203, 64, v202\n\t"
    "v_xor_b32 v205, 64, v204\n\t"
    "v_lshlrev_b32 v212, 4, v212\n\t"
    "v_lshl_add_u32 v212, v208, 9, v212\n\t"
    "v_lshl_add_u32 v212, v209, 3, v212\n\t"
    "v_add_u32 v206, s56, v212\n\t"
    "s_lshr_b32 s77, s71, 1\n\t"
    "s_lshl3_add_u32 s74, s72, s77\n\t"
    "s_lshl_b32 s74, s74, 19\n\t"
    "s_and_b32 s75, s70, 1\n\t"
    "s_lshl_b32 s75, s75, 12\n\t"
    "s_lshr_b32 s76, s70, 1\n\t"
    "s_lshl_b32 s76, s76, 10\n\t"
    "s_or_b32 s75, s75, s76\n\t"
    "s_or_b32 s74, s74, s75\n\t"
    "s_sub_u32 s75, 524288, s75\n\t"
    "s_add_u32 s28, s6, s74\n\t"
    "s_addc_u32 s29, s7, 0\n\t"
    "s_and_b32 s29, s29, 0xffff\n\t"
    "s_mov_b32 s30, s75\n\t"
    "s_mov_b32 s31, 0x20000\n\t"
    "v_bfe_u32 v211, v223, 3, 1\n\t"
    "v_lshlrev_b32 v240, 9, v211\n\t"
    "v_lshl_add_u32 v240, v209, 16, v240\n\t"
    "v_lshl_add_u32 v240, v208, 11, v240\n\t"
    "v_add_u32 v226, v240, v221\n\t"
    "v_xor_b32 v227, 64, v226\n\t"
    "v_add_u32 v227, 31744, v227\n\t"
    "v_add_u32 v228, 260096, v226\n\t"
    "v_add_u32 v229, 260096, v227\n\t"
    "s_add_u32 s32, s8, s74\n\t"
    "s_addc_u32 s33, s9, 0\n\t"
    "s_and_b32 s33, s33, 0xffff\n\t"
    "s_mov_b32 s34, s75\n\t"
    "s_mov_b32 s35, 0x20000\n\t"
    "s_and_b32 s76, s72, 7\n\t"
    "s_and_b32 s77, s77, 0\n\t"
    "s_xor_b32 s76, s76, s77\n\t"
    "s_mov_b32 s68, 0\n\t"
    "s_lshr_b32 s77, s76, 0\n\t"
    "s_and_b32 s77, s77, 1\n\t"
    "s_lshl_b32 s77, s77, 13\n\t"
    "s_or_b32 s68, s68, s77\n\t"
    "s_lshr_b32 s77, s76, 1\n\t"
    "s_and_b32 s77, s77, 1\n\t"
    "s_lshl_b32 s77, s77, 17\n\t"
    "s_or_b32 s68, s68, s77\n\t"
    "s_lshr_b32 s77, s76, 2\n\t"
    "s_and_b32 s77, s77, 1\n\t"
    "s_lshl_b32 s77, s77, 14\n\t"
    "s_or_b32 s68, s68, s77\n\t"
    "s_lshl3_add_u32 s74, s73, s72\n\t"
    "s_lshl_b32 s75, s74, 15\n\t"
    "s_mov_b32 s78, 16384\n\t"
    "s_add_u32 s44, s18, s75\n\t"
    "s_addc_u32 s45, s19, 0\n\t"
    "s_and_b32 s45, s45, 0xffff\n\t"
    "s_mov_b32 s46, 32768\n\t"
    "s_mov_b32 s47, 0x20000\n\t"
    "v_lshlrev_b32 v238, 4, v223\n\t"
    "v_and_b32 v213, 0, v223\n\t"
    "buffer_load_dwordx2 v[126:127], v213, s[44:47], s78 offen sc1\n\t"
    "s_mul_i32 s74, s74, 917504\n\t"
    "s_add_u32 s74, s74, 524288\n\t"
    "s_add_u32 s48, s18, s74\n\t"
    "s_addc_u32 s49, s19, 0\n\t"
    "s_and_b32 s49, s49, 0xffff\n\t"
    "s_mov_b32 s50, 917504\n\t"
    "s_mov_b32 s51, 0x20000\n\t"
    "s_mul_i32 s75, s71, 57344\n\t"
    "s_add_u32 s74, s74, s75\n\t"
    "s_lshl_b32 s75, s70, 11\n\t"
    "s_add_u32 s74, s74, s75\n\t"
    "s_sub_u32 s75, 57344, s75\n\t"
    "s_add_u32 s40, s18, s74\n\t"
    "s_addc_u32 s41, s19, 0\n\t"
    "s_and_b32 s41, s41, 0xffff\n\t"
    "s_mov_b32 s42, s75\n\t"
    "s_mov_b32 s43, 0x20000\n\t"
    "s_lshr_b32 s75, s72, 0\n\t"
    "s_and_b32 s75, s75, 3\n\t"
    "s_lshl_b32 s64, s75, 7\n\t"
    "s_or_b32 s64, s64, s68\n\t"
    "s_xor_b32 s75, s75, s70\n\t"
    "s_lshl2_add_u32 s74, s71, s75\n\t"
    "s_mul_i32 s74, s74, 57344\n\t"
    "s_add_u32 s36, s12, s74\n\t"
    "s_addc_u32 s37, s13, 0\n\t"
    "s_and_b32 s37, s37, 0xffff\n\t"
    "s_mov_b32 s38, 57344\n\t"
    "s_mov_b32 s39, 0x20000\n\t"
    "s_mul_i32 s74, s72, 114688\n\t"
    "s_mul_i32 s75, s73, 896\n\t"
    "s_add_u32 s74, s74, s75\n\t"
    "s_sub_u32 s75, 114688, s75\n\t"
    "s_add_u32 s52, s16, s74\n\t"
    "s_addc_u32 s53, s17, 0\n\t"
    "s_and_b32 s53, s53, 0xffff\n\t"
    "s_mov_b32 s54, s75\n\t"
    "s_mov_b32 s55, 0x20000\n\t"
    "s_mov_b32 s69, 0x3db8aa3b\n\t"
    "v_add_u32 v224, s56, v238\n\t"
    "v_and_b32 v211, 31, v223\n\t"
    "v_lshl_add_u32 v211, v211, 7, v214\n\t"
    "v_add_u32 v230, s56, v211\n\t"
    "v_xor_b32 v231, 32, v230\n\t"
    "v_xor_b32 v232, 64, v230\n\t"
    "v_xor_b32 v233, 96, v230\n\t"
    "s_and_b32 s75, s70, 1\n\t"
    "s_lshl_b32 s75, s75, 10\n\t"
    "s_add_u32 s75, s75, 32768\n\t"
    "v_add_u32 v215, s75, v238\n\t"
    "v_xor_b32 v217, 1024, v215\n\t"
    "v_add_u32 v216, 81920, v215\n\t"
    "v_add_u32 v218, 81920, v217\n\t"
    "v_add_u32 v240, v240, v222\n\t"
    "v_add_u32 v241, 31744, v240\n\t"
    "v_add_u32 v242, 260096, v240\n\t"
    "v_add_u32 v243, 291840, v240\n\t"
    "v_and_b32 v211, 3, v223\n\t"
    "v_lshl_add_u32 v212, v211, 3, v220\n\t"
    "v_lshl_add_u32 v212, v208, 9, v212\n\t"
    "s_and_b32 s75, s70, 2\n\t"
    "s_lshl_b32 s75, s75, 5\n\t"
    "v_xor_b32 v210, s75, v212\n\t"
    "v_add_u32 v244, s56, v210\n\t"
    "v_lshrrev_b32 v212, 2, v223\n\t"
    "v_lshlrev_b32 v212, 7, v212\n\t"
    "v_lshl_add_u32 v212, v211, 3, v212\n\t"
    "v_and_b32 v211, 3, v219\n\t"
    "v_xor_b32 v211, s70, v211\n\t"
    "v_lshl_add_u32 v250, v211, 5, v212\n\t"
    "v_lshlrev_b32 v211, 1, v207\n\t"
    "v_and_b32 v212, 7, v223\n\t"
    "v_xor_b32 v211, v211, v212\n\t"
    "v_mul_u32_u24 v212, 896, v219\n\t"
    "v_lshl_add_u32 v246, v211, 4, v212\n\t"
    "v_add_u32 v247, 6144, v246\n\t"
    "v_add_u32 v248, 12288, v246\n\t"
    "v_add_u32 v249, 18432, v246\n\t"
    "s_xor_b32 s75, s70, 1\n\t"
    "s_mul_i32 s75, s75, 40960\n\t"
    "v_add_u32 v251, s75, v238\n\t"
    "v_add_u32 v234, s75, v210\n\t"
    "s_xor_b32 s75, s70, 2\n\t"
    "s_mul_i32 s75, s75, 40960\n\t"
    "v_add_u32 v252, s75, v238\n\t"
    "v_add_u32 v235, s75, v210\n\t"
    "s_xor_b32 s75, s70, 3\n\t"
    "s_mul_i32 s75, s75, 40960\n\t"
    "v_add_u32 v253, s75, v238\n\t"
    "v_add_u32 v236, s75, v210\n\t"
    "v_add_u32 v254, 28672, v238\n\t"
    "v_add_u32 v255, 110592, v238\n\t"
    "v_and_b32 v211, 15, v223\n\t"
    "v_lshlrev_b32 v212, 6, v211\n\t"
    "v_lshl_add_u32 v225, v207, 4, v212\n\t"
    "v_lshl_add_u32 v211, v208, 4, v211\n\t"
    "v_lshlrev_b32 v211, 4, v211\n\t"
    "v_lshl_add_u32 v211, v209, 10, v211\n\t"
    "v_add_u32 v237, s56, v211\n\t"
    "s_add_u32 m0, s56, 4096\n\t"
    "s_add_u32 s80, s57, 262144\n\t"
    "buffer_load_dwordx4 v194, s[24:27], s80 offen lds\n\t"
    "buffer_load_dwordx4 v195, s[24:27], s80 offen offset:1024 lds\n\t"
    "buffer_load_dwordx4 v196, s[24:27], s80 offen offset:2048 lds\n\t"
    "buffer_load_dwordx4 v197, s[24:27], s80 offen offset:3072 lds\n\t"
    "s_add_u32 m0, s66, 26624\n\t"
    "s_lshr_b32 s80, s57, 12\n\t"
    "buffer_load_dwordx4 v193, s[20:23], s80 offen lds\n\t"
    "s_add_u32 m0, s66, 28672\n\t"
    "s_lshr_b32 s80, s58, 12\n\t"
    "buffer_load_dwordx4 v193, s[20:23], s80 offen lds\n\t"
    "s_add_u32 m0, s66, 30720\n\t"
    "s_lshr_b32 s80, s59, 12\n\t"
    "buffer_load_dwordx4 v193, s[20:23], s80 offen lds\n\t"
    "s_add_u32 m0, s66, 32768\n\t"
    "s_lshr_b32 s80, s60, 12\n\t"
    "buffer_load_dwordx4 v193, s[20:23], s80 offen lds\n\t"
    "s_add_u32 m0, s66, 34816\n\t"
    "s_lshr_b32 s80, s61, 12\n\t"
    "buffer_load_dwordx4 v193, s[20:23], s80 offen lds\n\t"
    "s_waitcnt vmcnt(10)\n\t"
    "s_waitcnt lgkmcnt(0)\n\t"
    "ds_read_b64_tr_b16 a[0:1], v198 offset:0\n\t"
    "ds_read_b64_tr_b16 a[2:3], v198 offset:512\n\t"
    "ds_read_b64_tr_b16 a[4:5], v199 offset:0\n\t"
    "ds_read_b64_tr_b16 a[6:7], v199 offset:512\n\t"
    "ds_read_b64_tr_b16 a[8:9], v200 offset:0\n\t"
    "ds_read_b64_tr_b16 a[10:11], v200 offset:512\n\t"
    "ds_read_b64_tr_b16 a[12:13], v201 offset:0\n\t"
    "ds_read_b64_tr_b16 a[14:15], v201 offset:512\n\t"
    "s_add_u32 m0, s66, 36864\n\t"
    "s_lshr_b32 s80, s62, 12\n\t"
    "buffer_load_dwordx4 v193, s[20:23], s80 offen lds\n\t"
    "s_add_u32 m0, s66, 38912\n\t"
    "s_lshr_b32 s80, s63, 12\n\t"
    "buffer_load_dwordx4 v193, s[20:23], s80 offen lds\n\t"
    "s_add_u32 m0, s56, 8192\n\t"
    "s_add_u32 s80, s58, 0\n\t"
    "buffer_load_dwordx4 v194, s[24:27], s80 offen lds\n\t"
    "buffer_load_dwordx4 v195, s[24:27], s80 offen offset:1024 lds\n\t"
    "buffer_load_dwordx4 v196, s[24:27], s80 offen offset:2048 lds\n\t"
    "s_waitcnt vmcnt(10)\n\t"
    "ds_read_b64_tr_b16 a[16:17], v198 offset:4096\n\t"
    "ds_read_b64_tr_b16 a[18:19], v198 offset:4608\n\t"
    "ds_read_b64_tr_b16 a[20:21], v199 offset:4096\n\t"
    "ds_read_b64_tr_b16 a[22:23], v199 offset:4608\n\t"
    "ds_read_b64_tr_b16 a[24:25], v200 offset:4096\n\t"
    "ds_read_b64_tr_b16 a[26:27], v200 offset:4608\n\t"
    "ds_read_b64_tr_b16 a[28:29], v201 offset:4096\n\t"
    "ds_read_b64_tr_b16 a[30:31], v201 offset:4608\n\t"
    "v_readfirstlane_b32 s84, v126\n\t"
    "v_readfirstlane_b32 s85, v127\n\t"
    "s_nop 3\n\t"
    "s_add_u32 s84, s84, 0x7F4A7C15\n\t"
    "s_addc_u32 s85, s85, 0x9E3779B9\n\t"
    "buffer_load_dwordx4 v197, s[24:27], s80 offen offset:3072 lds\n\t"
    "s_add_u32 m0, s56, 12288\n\t"
    "s_add_u32 s80, s58, 262144\n\t"
    "buffer_load_dwordx4 v194, s[24:27], s80 offen lds\n\t"
    "buffer_load_dwordx4 v195, s[24:27], s80 offen offset:1024 lds\n\t"
    "buffer_load_dwordx4 v196, s[24:27], s80 offen offset:2048 lds\n\t"
    "buffer_load_dwordx4 v197, s[24:27], s80 offen offset:3072 lds\n\t"
    "s_add_u32 m0, s56, 16384\n\t"
    "s_add_u32 s80, s59, 0\n\t"
    "buffer_load_dwordx4 v194, s[24:27], s80 offen lds\n\t"
    "buffer_load_dwordx4 v195, s[24:27], s80 offen offset:1024 lds\n\t"
    "s_waitcnt vmcnt(10)\n\t"
    "s_waitcnt lgkmcnt(0)\n\t"
    "s_barrier\n\t"
    "ds_read_b128 v[0:3], v202 offset:0\n\t"
    "ds_read_b128 v[4:7], v202 offset:40960\n\t"
    "ds_read_b128 v[8:11], v204 offset:0\n\t"
    "ds_read_b128 v[12:15], v204 offset:40960\n\t"
    "buffer_load_dwordx4 v196, s[24:27], s80 offen offset:2048 lds\n\t"
    "buffer_load_dwordx4 v197, s[24:27], s80 offen offset:3072 lds\n\t"
    "s_add_u32 m0, s56, 20480\n\t"
    "s_add_u32 s80, s59, 262144\n\t"
    "buffer_load_dwordx4 v194, s[24:27], s80 offen lds\n\t"
    "buffer_load_dwordx4 v195, s[24:27], s80 offen offset:1024 lds\n\t"
    "s_waitcnt vmcnt(10)\n\t"
    "ds_read_b64_tr_b16 a[32:33], v198 offset:8192\n\t"
    "ds_read_b64_tr_b16 a[34:35], v198 offset:8704\n\t"
    "ds_read_b64_tr_b16 a[36:37], v199 offset:8192\n\t"
    "ds_read_b64_tr_b16 a[38:39], v199 offset:8704\n\t"
    "ds_read_b64_tr_b16 a[40:41], v200 offset:8192\n\t"
    "ds_read_b64_tr_b16 a[42:43], v200 offset:8704\n\t"
    "ds_read_b64_tr_b16 a[44:45], v201 offset:8192\n\t"
    "ds_read_b64_tr_b16 a[46:47], v201 offset:8704\n\t"
    "ds_read_b128 v[16:19], v203 offset:0\n\t"
    "ds_read_b128 v[20:23], v203 offset:40960\n\t"
    "ds_read_b128 v[24:27], v205 offset:0\n\t"
    "ds_read_b128 v[28:31], v205 offset:40960\n\t"
    "buffer_load_dwordx4 v196, s[24:27], s80 offen offset:2048 lds\n\t"
    "buffer_load_dwordx4 v197, s[24:27], s80 offen offset:3072 lds\n\t"
    "s_add_u32 m0, s56, 0\n\t"
    "s_add_u32 s80, s60, 0\n\t"
    "buffer_load_dwordx4 v194, s[24:27], s80 offen lds\n\t"
    "buffer_load_dwordx4 v195, s[24:27], s80 offen offset:1024 lds\n\t"
    "s_waitcnt vmcnt(10)\n\t"
    "ds_read_b64_tr_b16 a[48:49], v198 offset:12288\n\t"
    "ds_read_b64_tr_b16 a[50:51], v198 offset:12800\n\t"
    "ds_read_b64_tr_b16 a[52:53], v199 offset:12288\n\t"
    "ds_read_b64_tr_b16 a[54:55], v199 offset:12800\n\t"
    "ds_read_b64_tr_b16 a[56:57], v200 offset:12288\n\t"
    "ds_read_b64_tr_b16 a[58:59], v200 offset:12800\n\t"
    "ds_read_b64_tr_b16 a[60:61], v201 offset:12288\n\t"
    "ds_read_b64_tr_b16 a[62:63], v201 offset:12800\n\t"
    "ds_read_b128 v[32:35], v202 offset:2048\n\t"
    "ds_read_b128 v[36:39], v202 offset:43008\n\t"
    "ds_read_b128 v[40:43], v204 offset:2048\n\t"
    "ds_read_b128 v[44:47], v204 offset:43008\n\t"
    "buffer_load_dwordx4 v196, s[24:27], s80 offen offset:2048 lds\n\t"
    "buffer_load_dwordx4 v197, s[24:27], s80 offen offset:3072 lds\n\t"
    "s_bitcmp1_b32 s72, 0\n\t"
    "s_cbranch_scc0 Lsa0_%=\n\t"
    "s_sleep 5\n\t"
    "Lsa0_%=:\n\t"
    "s_bitcmp1_b32 s72, 1\n\t"
    "s_cbranch_scc0 Lsa1_%=\n\t"
    "s_sleep 10\n\t"
    "Lsa1_%=:\n\t"
    "s_waitcnt lgkmcnt(14)\n\t"
    "v_mfma_f32_16x16x32_bf16 v[128:131], a[0:3], v[0:3], 0\n\t"
    "s_add_u32 m0, s56, 4096\n\t"
    "s_add_u32 s80, s60, 262144\n\t"
    "buffer_load_dwordx4 v194, s[24:27], s80 offen lds\n\t"
    "v_mfma_f32_16x16x32_bf16 v[132:135], a[0:3], v[4:7], 0\n\t"
    "ds_read_b128 v[48:51], v203 offset:2048\n\t"
    "v_mfma_f32_16x16x32_bf16 v[136:139], a[0:3], v[8:11], 0\n\t"
    "buffer_load_dwordx4 v195, s[24:27], s80 offen offset:1024 lds\n\t"
    "v_mfma_f32_16x16x32_bf16 v[140:143], a[0:3], v[12:15], 0\n\t"
    "ds_read_b128 v[52:55], v203 offset:43008\n\t"
    "v_mfma_f32_16x16x32_bf16 v[144:147], a[4:7], v[0:3], 0\n\t"
    "buffer_load_dwordx4 v196, s[24:27], s80 offen offset:2048 lds\n\t"
    "v_mfma_f32_16x16x32_bf16 v[148:151], a[4:7], v[4:7], 0\n\t"
    "ds_read_b128 v[56:59], v205 offset:2048\n\t"
    "v_mfma_f32_16x16x32_bf16 v[152:155], a[4:7], v[8:11], 0\n\t"
    "ds_read_b128 v[60:63], v205 offset:43008\n\t"
    "v_mfma_f32_16x16x32_bf16 v[156:159], a[4:7], v[12:15], 0\n\t"
    "s_waitcnt vmcnt(11)\n\t"
    "ds_read_b64_tr_b16 a[64:65], v198 offset:16384\n\t"
    "v_mfma_f32_16x16x32_bf16 v[160:163], a[8:11], v[0:3], 0\n\t"
    "buffer_load_dwordx4 v197, s[24:27], s80 offen offset:3072 lds\n\t"
    "v_mfma_f32_16x16x32_bf16 v[164:167], a[8:11], v[4:7], 0\n\t"
    "ds_read_b64_tr_b16 a[66:67], v198 offset:16896\n\t"
    "v_mfma_f32_16x16x32_bf16 v[168:171], a[8:11], v[8:11], 0\n\t"
    "ds_read_b64_tr_b16 a[68:69], v199 offset:16384\n\t"
    "v_mfma_f32_16x16x32_bf16 v[172:175], a[8:11], v[12:15], 0\n\t"
    "ds_read_b64_tr_b16 a[70:71], v199 offset:16896\n\t"
    "v_mfma_f32_16x16x32_bf16 v[176:179], a[12:15], v[0:3], 0\n\t"
    "ds_read_b64_tr_b16 a[72:73], v200 offset:16384\n\t"
    "v_mfma_f32_16x16x32_bf16 v[180:183], a[12:15], v[4:7], 0\n\t"
    "ds_read_b64_tr_b16 a[74:75], v200 offset:16896\n\t"
    "v_mfma_f32_16x16x32_bf16 v[184:187], a[12:15], v[8:11], 0\n\t"
    "ds_read_b64_tr_b16 a[76:77], v201 offset:16384\n\t"
    "v_mfma_f32_16x16x32_bf16 v[188:191], a[12:15], v[12:15], 0\n\t"
    "ds_read_b64_tr_b16 a[78:79], v201 offset:16896\n\t"
    "v_mfma_f32_16x16x32_bf16 v[128:131], a[16:19], v[16:19], v[128:131]\n\t"
    "s_add_u32 m0, s56, 8192\n\t"
    "s_add_u32 s80, s61, 0\n\t"
    "buffer_load_dwordx4 v194, s[24:27], s80 offen lds\n\t"
    "v_mfma_f32_16x16x32_bf16 v[132:135], a[16:19], v[20:23], v[132:135]\n\t"
    "ds_read_b128 v[64:67], v202 offset:4096\n\t"
    "s_waitcnt lgkmcnt(14)\n\t"
    "v_mfma_f32_16x16x32_bf16 v[136:139], a[16:19], v[24:27], v[136:139]\n\t"
    "buffer_load_dwordx4 v195, s[24:27], s80 offen offset:1024 lds\n\t"
    "v_mfma_f32_16x16x32_bf16 v[140:143], a[16:19], v[28:31], v[140:143]\n\t"
    "ds_read_b128 v[68:71], v202 offset:45056\n\t"
    "v_mfma_f32_16x16x32_bf16 v[144:147], a[20:23], v[16:19], v[144:147]\n\t"
    "buffer_load_dwordx4 v196, s[24:27], s80 offen offset:2048 lds\n\t"
    "v_mfma_f32_16x16x32_bf16 v[148:151], a[20:23], v[20:23], v[148:151]\n\t"
    "ds_read_b128 v[72:75], v204 offset:4096\n\t"
    "v_mfma_f32_16x16x32_bf16 v[152:155], a[20:23], v[24:27], v[152:155]\n\t"
    "ds_read_b128 v[76:79], v204 offset:45056\n\t"
    "v_mfma_f32_16x16x32_bf16 v[156:159], a[20:23], v[28:31], v[156:159]\n\t"
    "s_waitcnt vmcnt(11)\n\t"
    "ds_read_b64_tr_b16 a[80:81], v198 offset:20480\n\t"
    "v_mfma_f32_16x16x32_bf16 v[160:163], a[24:27], v[16:19], v[160:163]\n\t"
    "buffer_load_dwordx4 v197, s[24:27], s80 offen offset:3072 lds\n\t"
    "v_mfma_f32_16x16x32_bf16 v[164:167], a[24:27], v[20:23], v[164:167]\n\t"
    "ds_read_b64_tr_b16 a[82:83], v198 offset:20992\n\t"
    "v_mfma_f32_16x16x32_bf16 v[168:171], a[24:27], v[24:27], v[168:171]\n\t"
    "s_add_u32 m0, s56, 12288\n\t"
    "s_add_u32 s80, s61, 262144\n\t"
    "buffer_load_dwordx4 v194, s[24:27], s80 offen lds\n\t"
    "v_mfma_f32_16x16x32_bf16 v[172:175], a[24:27], v[28:31], v[172:175]\n\t"
    "ds_read_b64_tr_b16 a[84:85], v199 offset:20480\n\t"
    "v_mfma_f32_16x16x32_bf16 v[176:179], a[28:31], v[16:19], v[176:179]\n\t"
    "buffer_load_dwordx4 v195, s[24:27], s80 offen offset:1024 lds\n\t"
    "v_mfma_f32_16x16x32_bf16 v[180:183], a[28:31], v[20:23], v[180:183]\n\t"
    "ds_read_b64_tr_b16 a[86:87], v199 offset:20992\n\t"
    "v_mfma_f32_16x16x32_bf16 v[184:187], a[28:31], v[24:27], v[184:187]\n\t"
    "ds_read_b64_tr_b16 a[88:89], v200 offset:20480\n\t"
    "v_mfma_f32_16x16x32_bf16 v[188:191], a[28:31], v[28:31], v[188:191]\n\t"
    "ds_read_b64_tr_b16 a[90:91], v200 offset:20992\n\t"
    "v_mfma_f32_16x16x32_bf16 v[128:131], a[32:35], v[32:35], v[128:131]\n\t"
    "buffer_load_dwordx4 v196, s[24:27], s80 offen offset:2048 lds\n\t"
    "v_mfma_f32_16x16x32_bf16 v[132:135], a[32:35], v[36:39], v[132:135]\n\t"
    "ds_read_b64_tr_b16 a[92:93], v201 offset:20480\n\t"
    "v_mfma_f32_16x16x32_bf16 v[136:139], a[32:35], v[40:43], v[136:139]\n\t"
    "ds_read_b64_tr_b16 a[94:95], v201 offset:20992\n\t"
    "s_waitcnt lgkmcnt(14)\n\t"
    "v_mfma_f32_16x16x32_bf16 v[140:143], a[32:35], v[44:47], v[140:143]\n\t"
    "s_waitcnt vmcnt(11)\n\t"
    "ds_read_b64_tr_b16 a[96:97], v198 offset:0\n\t"
    "v_mfma_f32_16x16x32_bf16 v[144:147], a[36:39], v[32:35], v[144:147]\n\t"
    "buffer_load_dwordx4 v197, s[24:27], s80 offen offset:3072 lds\n\t"
    "v_mfma_f32_16x16x32_bf16 v[148:151], a[36:39], v[36:39], v[148:151]\n\t"
    "ds_read_b64_tr_b16 a[98:99], v198 offset:512\n\t"
    "v_mfma_f32_16x16x32_bf16 v[152:155], a[36:39], v[40:43], v[152:155]\n\t"
    "ds_read_b64_tr_b16 a[100:101], v199 offset:0\n\t"
    "v_mfma_f32_16x16x32_bf16 v[156:159], a[36:39], v[44:47], v[156:159]\n\t"
    "ds_read_b64_tr_b16 a[102:103], v199 offset:512\n\t"
    "v_mfma_f32_16x16x32_bf16 v[160:163], a[40:43], v[32:35], v[160:163]\n\t"
    "ds_read_b64_tr_b16 a[104:105], v200 offset:0\n\t"
    "v_mfma_f32_16x16x32_bf16 v[164:167], a[40:43], v[36:39], v[164:167]\n\t"
    "ds_read_b64_tr_b16 a[106:107], v200 offset:512\n\t"
    "v_mfma_f32_16x16x32_bf16 v[168:171], a[40:43], v[40:43], v[168:171]\n\t"
    "s_waitcnt lgkmcnt(14)\n\t"
    "s_add_u32 m0, s56, 16384\n\t"
    "s_add_u32 s80, s62, 0\n\t"
    "buffer_load_dwordx4 v194, s[24:27], s80 offen lds\n\t"
    "v_mfma_f32_16x16x32_bf16 v[172:175], a[40:43], v[44:47], v[172:175]\n\t"
    "ds_read_b64_tr_b16 a[108:109], v201 offset:0\n\t"
    "v_mfma_f32_16x16x32_bf16 v[176:179], a[44:47], v[32:35], v[176:179]\n\t"
    "ds_read_b64_tr_b16 a[110:111], v201 offset:512\n\t"
    "v_mfma_f32_16x16x32_bf16 v[180:183], a[44:47], v[36:39], v[180:183]\n\t"
    "ds_read_b128 v[80:83], v203 offset:4096\n\t"
    "v_mfma_f32_16x16x32_bf16 v[184:187], a[44:47], v[40:43], v[184:187]\n\t"
    "buffer_load_dwordx4 v195, s[24:27], s80 offen offset:1024 lds\n\t"
    "v_mfma_f32_16x16x32_bf16 v[188:191], a[44:47], v[44:47], v[188:191]\n\t"
    "ds_read_b128 v[84:87], v203 offset:45056\n\t"
    "v_mfma_f32_16x16x32_bf16 v[128:131], a[48:51], v[48:51], v[128:131]\n\t"
    "ds_read_b128 v[88:91], v205 offset:4096\n\t"
    "v_mfma_f32_16x16x32_bf16 v[132:135], a[48:51], v[52:55], v[132:135]\n\t"
    "ds_read_b128 v[92:95], v205 offset:45056\n\t"
    "v_mfma_f32_16x16x32_bf16 v[136:139], a[48:51], v[56:59], v[136:139]\n\t"
    "buffer_load_dwordx4 v196, s[24:27], s80 offen offset:2048 lds\n\t"
    "v_mfma_f32_16x16x32_bf16 v[140:143], a[48:51], v[60:63], v[140:143]\n\t"
    "ds_read_b128 v[96:99], v202 offset:6144\n\t"
    "v_mfma_f32_16x16x32_bf16 v[144:147], a[52:55], v[48:51], v[144:147]\n\t"
    "ds_read_b128 v[100:103], v202 offset:47104\n\t"
    "v_mfma_f32_16x16x32_bf16 v[148:151], a[52:55], v[52:55], v[148:151]\n\t"
    "ds_read_b128 v[104:107], v204 offset:6144\n\t"
    "v_mfma_f32_16x16x32_bf16 v[152:155], a[52:55], v[56:59], v[152:155]\n\t"
    "buffer_load_dwordx4 v197, s[24:27], s80 offen offset:3072 lds\n\t"
    "v_mfma_f32_16x16x32_bf16 v[156:159], a[52:55], v[60:63], v[156:159]\n\t"
    "ds_read_b128 v[108:111], v204 offset:47104\n\t"
    "v_mfma_f32_16x16x32_bf16 v[160:163], a[56:59], v[48:51], v[160:163]\n\t"
    "s_waitcnt vmcnt(12)\n\t"
    "ds_read_b64_tr_b16 a[112:113], v198 offset:4096\n\t"
    "v_mfma_f32_16x16x32_bf16 v[164:167], a[56:59], v[52:55], v[164:167]\n\t"
    "ds_read_b64_tr_b16 a[114:115], v198 offset:4608\n\t"
    "v_mfma_f32_16x16x32_bf16 v[168:171], a[56:59], v[56:59], v[168:171]\n\t"
    "s_waitcnt lgkmcnt(14)\n\t"
    "s_add_u32 m0, s56, 20480\n\t"
    "s_add_u32 s80, s62, 262144\n\t"
    "buffer_load_dwordx4 v194, s[24:27], s80 offen lds\n\t"
    "v_mfma_f32_16x16x32_bf16 v[172:175], a[56:59], v[60:63], v[172:175]\n\t"
    "ds_read_b64_tr_b16 a[116:117], v199 offset:4096\n\t"
    "v_mfma_f32_16x16x32_bf16 v[176:179], a[60:63], v[48:51], v[176:179]\n\t"
    "ds_read_b64_tr_b16 a[118:119], v199 offset:4608\n\t"
    "v_mfma_f32_16x16x32_bf16 v[180:183], a[60:63], v[52:55], v[180:183]\n\t"
    "ds_read_b64_tr_b16 a[120:121], v200 offset:4096\n\t"
    "v_mfma_f32_16x16x32_bf16 v[184:187], a[60:63], v[56:59], v[184:187]\n\t"
    "buffer_load_dwordx4 v195, s[24:27], s80 offen offset:1024 lds\n\t"
    "v_mfma_f32_16x16x32_bf16 v[188:191], a[60:63], v[60:63], v[188:191]\n\t"
    "ds_read_b64_tr_b16 a[122:123], v200 offset:4608\n\t"
    "v_mfma_f32_16x16x32_bf16 v[128:131], a[64:67], v[64:67], v[128:131]\n\t"
    "ds_read_b64_tr_b16 a[124:125], v201 offset:4096\n\t"
    "v_mfma_f32_16x16x32_bf16 v[132:135], a[64:67], v[68:71], v[132:135]\n\t"
    "ds_read_b64_tr_b16 a[126:127], v201 offset:4608\n\t"
    "v_mfma_f32_16x16x32_bf16 v[136:139], a[64:67], v[72:75], v[136:139]\n\t"
    "buffer_load_dwordx4 v196, s[24:27], s80 offen offset:2048 lds\n\t"
    "v_mfma_f32_16x16x32_bf16 v[140:143], a[64:67], v[76:79], v[140:143]\n\t"
    "ds_read_b128 v[112:115], v203 offset:6144\n\t"
    "v_mfma_f32_16x16x32_bf16 v[144:147], a[68:71], v[64:67], v[144:147]\n\t"
    "ds_read_b128 v[116:119], v203 offset:47104\n\t"
    "v_mfma_f32_16x16x32_bf16 v[148:151], a[68:71], v[68:71], v[148:151]\n\t"
    "ds_read_b128 v[120:123], v205 offset:6144\n\t"
    "v_mfma_f32_16x16x32_bf16 v[152:155], a[68:71], v[72:75], v[152:155]\n\t"
    "buffer_load_dwordx4 v197, s[24:27], s80 offen offset:3072 lds\n\t"
    "v_mfma_f32_16x16x32_bf16 v[156:159], a[68:71], v[76:79], v[156:159]\n\t"
    "ds_read_b128 v[124:127], v205 offset:47104\n\t"
    "v_mfma_f32_16x16x32_bf16 v[160:163], a[72:75], v[64:67], v[160:163]\n\t"
    "s_waitcnt vmcnt(12)\n\t"
    "ds_read_b64_tr_b16 a[128:129], v198 offset:8192\n\t"
    "v_mfma_f32_16x16x32_bf16 v[164:167], a[72:75], v[68:71], v[164:167]\n\t"
    "ds_read_b64_tr_b16 a[130:131], v198 offset:8704\n\t"
    "v_mfma_f32_16x16x32_bf16 v[168:171], a[72:75], v[72:75], v[168:171]\n\t"
    "s_waitcnt lgkmcnt(14)\n\t"
    "s_add_u32 m0, s56, 0\n\t"
    "s_add_u32 s80, s63, 0\n\t"
    "buffer_load_dwordx4 v194, s[24:27], s80 offen lds\n\t"
    "v_mfma_f32_16x16x32_bf16 v[172:175], a[72:75], v[76:79], v[172:175]\n\t"
    "ds_read_b64_tr_b16 a[132:133], v199 offset:8192\n\t"
    "v_mfma_f32_16x16x32_bf16 v[176:179], a[76:79], v[64:67], v[176:179]\n\t"
    "buffer_load_dwordx4 v195, s[24:27], s80 offen offset:1024 lds\n\t"
    "v_mfma_f32_16x16x32_bf16 v[180:183], a[76:79], v[68:71], v[180:183]\n\t"
    "ds_read_b64_tr_b16 a[134:135], v199 offset:8704\n\t"
    "v_mfma_f32_16x16x32_bf16 v[184:187], a[76:79], v[72:75], v[184:187]\n\t"
    "ds_read_b64_tr_b16 a[136:137], v200 offset:8192\n\t"
    "v_mfma_f32_16x16x32_bf16 v[188:191], a[76:79], v[76:79], v[188:191]\n\t"
    "ds_read_b64_tr_b16 a[138:139], v200 offset:8704\n\t"
    "v_mfma_f32_16x16x32_bf16 v[128:131], a[80:83], v[80:83], v[128:131]\n\t"
    "ds_read_b64_tr_b16 a[140:141], v201 offset:8192\n\t"
    "v_mfma_f32_16x16x32_bf16 v[132:135], a[80:83], v[84:87], v[132:135]\n\t"
    "ds_read_b64_tr_b16 a[142:143], v201 offset:8704\n\t"
    "v_mfma_f32_16x16x32_bf16 v[136:139], a[80:83], v[88:91], v[136:139]\n\t"
    "buffer_load_dwordx4 v196, s[24:27], s80 offen offset:2048 lds\n\t"
    "v_mfma_f32_16x16x32_bf16 v[140:143], a[80:83], v[92:95], v[140:143]\n\t"
    "s_waitcnt vmcnt(11)\n\t"
    "ds_read_b64_tr_b16 a[144:145], v198 offset:12288\n\t"
    "v_mfma_f32_16x16x32_bf16 v[144:147], a[84:87], v[80:83], v[144:147]\n\t"
    "ds_read_b64_tr_b16 a[146:147], v198 offset:12800\n\t"
    "v_mfma_f32_16x16x32_bf16 v[148:151], a[84:87], v[84:87], v[148:151]\n\t"
    "ds_read_b64_tr_b16 a[148:149], v199 offset:12288\n\t"
    "v_mfma_f32_16x16x32_bf16 v[152:155], a[84:87], v[88:91], v[152:155]\n\t"
    "buffer_load_dwordx4 v197, s[24:27], s80 offen offset:3072 lds\n\t"
    "v_mfma_f32_16x16x32_bf16 v[156:159], a[84:87], v[92:95], v[156:159]\n\t"
    "ds_read_b64_tr_b16 a[150:151], v199 offset:12800\n\t"
    "v_mfma_f32_16x16x32_bf16 v[160:163], a[88:91], v[80:83], v[160:163]\n\t"
    "ds_read_b64_tr_b16 a[152:153], v200 offset:12288\n\t"
    "v_mfma_f32_16x16x32_bf16 v[164:167], a[88:91], v[84:87], v[164:167]\n\t"
    "ds_read_b64_tr_b16 a[154:155], v200 offset:12800\n\t"
    "v_mfma_f32_16x16x32_bf16 v[168:171], a[88:91], v[88:91], v[168:171]\n\t"
    "s_waitcnt lgkmcnt(14)\n\t"
    "s_add_u32 m0, s56, 4096\n\t"
    "s_add_u32 s80, s63, 262144\n\t"
    "buffer_load_dwordx4 v194, s[24:27], s80 offen lds\n\t"
    "v_mfma_f32_16x16x32_bf16 v[172:175], a[88:91], v[92:95], v[172:175]\n\t"
    "ds_read_b64_tr_b16 a[156:157], v201 offset:12288\n\t"
    "v_mfma_f32_16x16x32_bf16 v[176:179], a[92:95], v[80:83], v[176:179]\n\t"
    "buffer_load_dwordx4 v195, s[24:27], s80 offen offset:1024 lds\n\t"
    "v_mfma_f32_16x16x32_bf16 v[180:183], a[92:95], v[84:87], v[180:183]\n\t"
    "ds_read_b64_tr_b16 a[158:159], v201 offset:12800\n\t"
    "v_mfma_f32_16x16x32_bf16 v[184:187], a[92:95], v[88:91], v[184:187]\n\t"
    "ds_read_b128 v[0:3], v202 offset:8192\n\t"
    "v_mfma_f32_16x16x32_bf16 v[188:191], a[92:95], v[92:95], v[188:191]\n\t"
    "ds_read_b128 v[4:7], v202 offset:49152\n\t"
    "v_mfma_f32_16x16x32_bf16 v[128:131], a[96:99], v[96:99], v[128:131]\n\t"
    "buffer_load_dwordx4 v196, s[24:27], s80 offen offset:2048 lds\n\t"
    "v_mfma_f32_16x16x32_bf16 v[132:135], a[96:99], v[100:103], v[132:135]\n\t"
    "ds_read_b128 v[8:11], v204 offset:8192\n\t"
    "v_mfma_f32_16x16x32_bf16 v[136:139], a[96:99], v[104:107], v[136:139]\n\t"
    "ds_read_b128 v[12:15], v204 offset:49152\n\t"
    "v_mfma_f32_16x16x32_bf16 v[140:143], a[96:99], v[108:111], v[140:143]\n\t"
    "s_waitcnt vmcnt(11)\n\t"
    "ds_read_b64_tr_b16 a[160:161], v198 offset:16384\n\t"
    "v_mfma_f32_16x16x32_bf16 v[144:147], a[100:103], v[96:99], v[144:147]\n\t"
    "buffer_load_dwordx4 v197, s[24:27], s80 offen offset:3072 lds\n\t"
    "v_mfma_f32_16x16x32_bf16 v[148:151], a[100:103], v[100:103], v[148:151]\n\t"
    "ds_read_b64_tr_b16 a[162:163], v198 offset:16896\n\t"
    "v_mfma_f32_16x16x32_bf16 v[152:155], a[100:103], v[104:107], v[152:155]\n\t"
    "ds_read_b64_tr_b16 a[164:165], v199 offset:16384\n\t"
    "v_mfma_f32_16x16x32_bf16 v[156:159], a[100:103], v[108:111], v[156:159]\n\t"
    "ds_read_b64_tr_b16 a[166:167], v199 offset:16896\n\t"
    "v_mfma_f32_16x16x32_bf16 v[160:163], a[104:107], v[96:99], v[160:163]\n\t"
    "s_waitcnt lgkmcnt(14)\n\t"
    "s_add_u32 m0, s56, 8192\n\t"
    "s_xor_b32 s80, s68, 0\n\t"
    "buffer_load_dwordx4 v226, s[28:31], s80 offen nt lds\n\t"
    "v_mfma_f32_16x16x32_bf16 v[164:167], a[104:107], v[100:103], v[164:167]\n\t"
    "ds_read_b64_tr_b16 a[168:169], v200 offset:16384\n\t"
    "v_mfma_f32_16x16x32_bf16 v[168:171], a[104:107], v[104:107], v[168:171]\n\t"
    "ds_read_b64_tr_b16 a[170:171], v200 offset:16896\n\t"
    "v_mfma_f32_16x16x32_bf16 v[172:175], a[104:107], v[108:111], v[172:175]\n\t"
    "ds_read_b64_tr_b16 a[172:173], v201 offset:16384\n\t"
    "v_mfma_f32_16x16x32_bf16 v[176:179], a[108:111], v[96:99], v[176:179]\n\t"
    "buffer_load_dwordx4 v227, s[28:31], s80 offen offset:1024 nt lds\n\t"
    "v_mfma_f32_16x16x32_bf16 v[180:183], a[108:111], v[100:103], v[180:183]\n\t"
    "ds_read_b64_tr_b16 a[174:175], v201 offset:16896\n\t"
    "v_mfma_f32_16x16x32_bf16 v[184:187], a[108:111], v[104:107], v[184:187]\n\t"
    "ds_read_b128 v[16:19], v203 offset:8192\n\t"
    "v_mfma_f32_16x16x32_bf16 v[188:191], a[108:111], v[108:111], v[188:191]\n\t"
    "ds_read_b128 v[20:23], v203 offset:49152\n\t"
    "v_mfma_f32_16x16x32_bf16 v[128:131], a[112:115], v[112:115], v[128:131]\n\t"
    "buffer_load_dwordx4 v228, s[28:31], s80 offen offset:2048 nt lds\n\t"
    "v_mfma_f32_16x16x32_bf16 v[132:135], a[112:115], v[116:119], v[132:135]\n\t"
    "ds_read_b128 v[24:27], v205 offset:8192\n\t"
    "v_mfma_f32_16x16x32_bf16 v[136:139], a[112:115], v[120:123], v[136:139]\n\t"
    "ds_read_b128 v[28:31], v205 offset:49152\n\t"
    "v_mfma_f32_16x16x32_bf16 v[140:143], a[112:115], v[124:127], v[140:143]\n\t"
    "s_waitcnt vmcnt(11)\n\t"
    "ds_read_b64_tr_b16 a[176:177], v198 offset:20480\n\t"
    "v_mfma_f32_16x16x32_bf16 v[144:147], a[116:119], v[112:115], v[144:147]\n\t"
    "buffer_load_dwordx4 v229, s[28:31], s80 offen offset:3072 nt lds\n\t"
    "v_mfma_f32_16x16x32_bf16 v[148:151], a[116:119], v[116:119], v[148:151]\n\t"
    "ds_read_b64_tr_b16 a[178:179], v198 offset:20992\n\t"
    "v_mfma_f32_16x16x32_bf16 v[152:155], a[116:119], v[120:123], v[152:155]\n\t"
    "ds_read_b64_tr_b16 a[180:181], v199 offset:20480\n\t"
    "v_mfma_f32_16x16x32_bf16 v[156:159], a[116:119], v[124:127], v[156:159]\n\t"
    "ds_read_b64_tr_b16 a[182:183], v199 offset:20992\n\t"
    "v_mfma_f32_16x16x32_bf16 v[160:163], a[120:123], v[112:115], v[160:163]\n\t"
    "s_waitcnt lgkmcnt(14)\n\t"
    "s_add_u32 m0, s56, 12288\n\t"
    "s_xor_b32 s80, s68, 128\n\t"
    "buffer_load_dwordx4 v226, s[28:31], s80 offen nt lds\n\t"
    "v_mfma_f32_16x16x32_bf16 v[164:167], a[120:123], v[116:119], v[164:167]\n\t"
    "ds_read_b64_tr_b16 a[184:185], v200 offset:20480\n\t"
    "v_mfma_f32_16x16x32_bf16 v[168:171], a[120:123], v[120:123], v[168:171]\n\t"
    "ds_read_b64_tr_b16 a[186:187], v200 offset:20992\n\t"
    "v_mfma_f32_16x16x32_bf16 v[172:175], a[120:123], v[124:127], v[172:175]\n\t"
    "ds_read_b64_tr_b16 a[188:189], v201 offset:20480\n\t"
    "v_mfma_f32_16x16x32_bf16 v[176:179], a[124:127], v[112:115], v[176:179]\n\t"
    "buffer_load_dwordx4 v227, s[28:31], s80 offen offset:1024 nt lds\n\t"
    "v_mfma_f32_16x16x32_bf16 v[180:183], a[124:127], v[116:119], v[180:183]\n\t"
    "ds_read_b64_tr_b16 a[190:191], v201 offset:20992\n\t"
    "v_mfma_f32_16x16x32_bf16 v[184:187], a[124:127], v[120:123], v[184:187]\n\t"
    "ds_read_b128 v[32:35], v202 offset:10240\n\t"
    "v_mfma_f32_16x16x32_bf16 v[188:191], a[124:127], v[124:127], v[188:191]\n\t"
    "ds_read_b128 v[36:39], v202 offset:51200\n\t"
    "v_mfma_f32_16x16x32_bf16 v[128:131], a[128:131], v[0:3], v[128:131]\n\t"
    "buffer_load_dwordx4 v228, s[28:31], s80 offen offset:2048 nt lds\n\t"
    "v_mfma_f32_16x16x32_bf16 v[132:135], a[128:131], v[4:7], v[132:135]\n\t"
    "ds_read_b128 v[40:43], v204 offset:10240\n\t"
    "v_mfma_f32_16x16x32_bf16 v[136:139], a[128:131], v[8:11], v[136:139]\n\t"
    "ds_read_b128 v[44:47], v204 offset:51200\n\t"
    "v_mfma_f32_16x16x32_bf16 v[140:143], a[128:131], v[12:15], v[140:143]\n\t"
    "s_waitcnt vmcnt(11)\n\t"
    "ds_read_b64_tr_b16 a[192:193], v198 offset:0\n\t"
    "v_mfma_f32_16x16x32_bf16 v[144:147], a[132:135], v[0:3], v[144:147]\n\t"
    "buffer_load_dwordx4 v229, s[28:31], s80 offen offset:3072 nt lds\n\t"
    "v_mfma_f32_16x16x32_bf16 v[148:151], a[132:135], v[4:7], v[148:151]\n\t"
    "ds_read_b64_tr_b16 a[194:195], v198 offset:512\n\t"
    "v_mfma_f32_16x16x32_bf16 v[152:155], a[132:135], v[8:11], v[152:155]\n\t"
    "ds_read_b64_tr_b16 a[196:197], v199 offset:0\n\t"
    "v_mfma_f32_16x16x32_bf16 v[156:159], a[132:135], v[12:15], v[156:159]\n\t"
    "ds_read_b64_tr_b16 a[198:199], v199 offset:512\n\t"
    "v_mfma_f32_16x16x32_bf16 v[160:163], a[136:139], v[0:3], v[160:163]\n\t"
    "s_waitcnt lgkmcnt(14)\n\t"
    "s_add_u32 m0, s56, 16384\n\t"
    "s_xor_b32 s80, s68, 256\n\t"
    "buffer_load_dwordx4 v226, s[28:31], s80 offen nt lds\n\t"
    "v_mfma_f32_16x16x32_bf16 v[164:167], a[136:139], v[4:7], v[164:167]\n\t"
    "ds_read_b64_tr_b16 a[200:201], v200 offset:0\n\t"
    "v_mfma_f32_16x16x32_bf16 v[168:171], a[136:139], v[8:11], v[168:171]\n\t"
    "ds_read_b64_tr_b16 a[202:203], v200 offset:512\n\t"
    "v_mfma_f32_16x16x32_bf16 v[172:175], a[136:139], v[12:15], v[172:175]\n\t"
    "ds_read_b64_tr_b16 a[204:205], v201 offset:0\n\t"
    "v_mfma_f32_16x16x32_bf16 v[176:179], a[140:143], v[0:3], v[176:179]\n\t"
    "buffer_load_dwordx4 v227, s[28:31], s80 offen offset:1024 nt lds\n\t"
    "v_mfma_f32_16x16x32_bf16 v[180:183], a[140:143], v[4:7], v[180:183]\n\t"
    "ds_read_b64_tr_b16 a[206:207], v201 offset:512\n\t"
    "v_mfma_f32_16x16x32_bf16 v[184:187], a[140:143], v[8:11], v[184:187]\n\t"
    "ds_read_b128 v[48:51], v203 offset:10240\n\t"
    "v_mfma_f32_16x16x32_bf16 v[188:191], a[140:143], v[12:15], v[188:191]\n\t"
    "ds_read_b128 v[52:55], v203 offset:51200\n\t"
    "v_mfma_f32_16x16x32_bf16 v[128:131], a[144:147], v[16:19], v[128:131]\n\t"
    "buffer_load_dwordx4 v228, s[28:31], s80 offen offset:2048 nt lds\n\t"
    "v_mfma_f32_16x16x32_bf16 v[132:135], a[144:147], v[20:23], v[132:135]\n\t"
    "ds_read_b128 v[56:59], v205 offset:10240\n\t"
    "v_mfma_f32_16x16x32_bf16 v[136:139], a[144:147], v[24:27], v[136:139]\n\t"
    "ds_read_b128 v[60:63], v205 offset:51200\n\t"
    "v_mfma_f32_16x16x32_bf16 v[140:143], a[144:147], v[28:31], v[140:143]\n\t"
    "s_waitcnt vmcnt(11)\n\t"
    "ds_read_b64_tr_b16 a[208:209], v198 offset:4096\n\t"
    "v_mfma_f32_16x16x32_bf16 v[144:147], a[148:151], v[16:19], v[144:147]\n\t"
    "ds_read_b64_tr_b16 a[210:211], v198 offset:4608\n\t"
    "v_mfma_f32_16x16x32_bf16 v[148:151], a[148:151], v[20:23], v[148:151]\n\t"
    "ds_read_b64_tr_b16 a[212:213], v199 offset:4096\n\t"
    "v_mfma_f32_16x16x32_bf16 v[152:155], a[148:151], v[24:27], v[152:155]\n\t"
    "ds_read_b64_tr_b16 a[214:215], v199 offset:4608\n\t"
    "v_mfma_f32_16x16x32_bf16 v[156:159], a[148:151], v[28:31], v[156:159]\n\t"
    "ds_read_b64_tr_b16 a[216:217], v200 offset:4096\n\t"
    "v_mfma_f32_16x16x32_bf16 v[160:163], a[152:155], v[16:19], v[160:163]\n\t"
    "ds_read_b64_tr_b16 a[218:219], v200 offset:4608\n\t"
    "v_mfma_f32_16x16x32_bf16 v[164:167], a[152:155], v[20:23], v[164:167]\n\t"
    "ds_read_b64_tr_b16 a[220:221], v201 offset:4096\n\t"
    "v_mfma_f32_16x16x32_bf16 v[168:171], a[152:155], v[24:27], v[168:171]\n\t"
    "ds_read_b64_tr_b16 a[222:223], v201 offset:4608\n\t"
    "v_mfma_f32_16x16x32_bf16 v[172:175], a[152:155], v[28:31], v[172:175]\n\t"
    "ds_read_b128 v[64:67], v202 offset:12288\n\t"
    "v_mfma_f32_16x16x32_bf16 v[176:179], a[156:159], v[16:19], v[176:179]\n\t"
    "buffer_load_dwordx4 v229, s[28:31], s80 offen offset:3072 nt lds\n\t"
    "v_mfma_f32_16x16x32_bf16 v[180:183], a[156:159], v[20:23], v[180:183]\n\t"
    "ds_read_b128 v[68:71], v202 offset:53248\n\t"
    "v_mfma_f32_16x16x32_bf16 v[184:187], a[156:159], v[24:27], v[184:187]\n\t"
    "ds_read_b128 v[72:75], v204 offset:12288\n\t"
    "v_mfma_f32_16x16x32_bf16 v[188:191], a[156:159], v[28:31], v[188:191]\n\t"
    "ds_read_b128 v[76:79], v204 offset:53248\n\t"
    "s_waitcnt lgkmcnt(14)\n\t"
    "v_mfma_f32_16x16x32_bf16 v[128:131], a[160:163], v[32:35], v[128:131]\n\t"
    "ds_read_b128 v[80:83], v203 offset:12288\n\t"
    "v_mfma_f32_16x16x32_bf16 v[132:135], a[160:163], v[36:39], v[132:135]\n\t"
    "ds_read_b128 v[84:87], v203 offset:53248\n\t"
    "v_mfma_f32_16x16x32_bf16 v[136:139], a[160:163], v[40:43], v[136:139]\n\t"
    "s_add_u32 m0, s56, 20480\n\t"
    "s_xor_b32 s80, s68, 384\n\t"
    "buffer_load_dwordx4 v226, s[28:31], s80 offen nt lds\n\t"
    "v_mfma_f32_16x16x32_bf16 v[140:143], a[160:163], v[44:47], v[140:143]\n\t"
    "ds_read_b128 v[88:91], v205 offset:12288\n\t"
    "v_mfma_f32_16x16x32_bf16 v[144:147], a[164:167], v[32:35], v[144:147]\n\t"
    "ds_read_b128 v[92:95], v205 offset:53248\n\t"
    "v_mfma_f32_16x16x32_bf16 v[148:151], a[164:167], v[36:39], v[148:151]\n\t"
    "v_mfma_f32_16x16x32_bf16 v[152:155], a[164:167], v[40:43], v[152:155]\n\t"
    "v_mfma_f32_16x16x32_bf16 v[156:159], a[164:167], v[44:47], v[156:159]\n\t"
    "v_mfma_f32_16x16x32_bf16 v[160:163], a[168:171], v[32:35], v[160:163]\n\t"
    "buffer_load_dwordx4 v227, s[28:31], s80 offen offset:1024 nt lds\n\t"
    "v_mfma_f32_16x16x32_bf16 v[164:167], a[168:171], v[36:39], v[164:167]\n\t"
    "v_mfma_f32_16x16x32_bf16 v[168:171], a[168:171], v[40:43], v[168:171]\n\t"
    "v_mfma_f32_16x16x32_bf16 v[172:175], a[168:171], v[44:47], v[172:175]\n\t"
    "v_mfma_f32_16x16x32_bf16 v[176:179], a[172:175], v[32:35], v[176:179]\n\t"
    "v_mfma_f32_16x16x32_bf16 v[180:183], a[172:175], v[36:39], v[180:183]\n\t"
    "v_mfma_f32_16x16x32_bf16 v[184:187], a[172:175], v[40:43], v[184:187]\n\t"
    "buffer_load_dwordx4 v228, s[28:31], s80 offen offset:2048 nt lds\n\t"
    "v_mfma_f32_16x16x32_bf16 v[188:191], a[172:175], v[44:47], v[188:191]\n\t"
    "s_waitcnt lgkmcnt(0)\n\t"
    "s_barrier\n\t"
    "v_mfma_f32_16x16x32_bf16 v[128:131], a[176:179], v[48:51], v[128:131]\n\t"
    "buffer_load_dwordx4 v229, s[28:31], s80 offen offset:3072 nt lds\n\t"
    "v_mfma_f32_16x16x32_bf16 v[132:135], a[176:179], v[52:55], v[132:135]\n\t"
    "v_mfma_f32_16x16x32_bf16 v[136:139], a[176:179], v[56:59], v[136:139]\n\t"
    "v_mfma_f32_16x16x32_bf16 v[140:143], a[176:179], v[60:63], v[140:143]\n\t"
    "v_mfma_f32_16x16x32_bf16 v[144:147], a[180:183], v[48:51], v[144:147]\n\t"
    "v_mfma_f32_16x16x32_bf16 v[148:151], a[180:183], v[52:55], v[148:151]\n\t"
    "v_mfma_f32_16x16x32_bf16 v[152:155], a[180:183], v[56:59], v[152:155]\n\t"
    "s_add_u32 m0, s56, 0\n\t"
    "s_xor_b32 s80, s68, 8192\n\t"
    "buffer_load_dwordx4 v226, s[28:31], s80 offen nt lds\n\t"
    "v_mfma_f32_16x16x32_bf16 v[156:159], a[180:183], v[60:63], v[156:159]\n\t"
    "v_mfma_f32_16x16x32_bf16 v[160:163], a[184:187], v[48:51], v[160:163]\n\t"
    "v_mfma_f32_16x16x32_bf16 v[164:167], a[184:187], v[52:55], v[164:167]\n\t"
    "v_mfma_f32_16x16x32_bf16 v[168:171], a[184:187], v[56:59], v[168:171]\n\t"
    "v_mfma_f32_16x16x32_bf16 v[172:175], a[184:187], v[60:63], v[172:175]\n\t"
    "v_mfma_f32_16x16x32_bf16 v[176:179], a[188:191], v[48:51], v[176:179]\n\t"
    "buffer_load_dwordx4 v227, s[28:31], s80 offen offset:1024 nt lds\n\t"
    "v_mfma_f32_16x16x32_bf16 v[180:183], a[188:191], v[52:55], v[180:183]\n\t"
    "v_mfma_f32_16x16x32_bf16 v[184:187], a[188:191], v[56:59], v[184:187]\n\t"
    "v_mfma_f32_16x16x32_bf16 v[188:191], a[188:191], v[60:63], v[188:191]\n\t"
    "v_mfma_f32_16x16x32_bf16 v[128:131], a[192:195], v[64:67], v[128:131]\n\t"
    "v_mfma_f32_16x16x32_bf16 v[132:135], a[192:195], v[68:71], v[132:135]\n\t"
    "v_mfma_f32_16x16x32_bf16 v[136:139], a[192:195], v[72:75], v[136:139]\n\t"
    "buffer_load_dwordx4 v228, s[28:31], s80 offen offset:2048 nt lds\n\t"
    "v_mfma_f32_16x16x32_bf16 v[140:143], a[192:195], v[76:79], v[140:143]\n\t"
    "v_mfma_f32_16x16x32_bf16 v[144:147], a[196:199], v[64:67], v[144:147]\n\t"
    "v_mfma_f32_16x16x32_bf16 v[148:151], a[196:199], v[68:71], v[148:151]\n\t"
    "v_mfma_f32_16x16x32_bf16 v[152:155], a[196:199], v[72:75], v[152:155]\n\t"
    "v_mfma_f32_16x16x32_bf16 v[156:159], a[196:199], v[76:79], v[156:159]\n\t"
    "v_mfma_f32_16x16x32_bf16 v[160:163], a[200:203], v[64:67], v[160:163]\n\t"
    "buffer_load_dwordx4 v229, s[28:31], s80 offen offset:3072 nt lds\n\t"
    "v_mfma_f32_16x16x32_bf16 v[164:167], a[200:203], v[68:71], v[164:167]\n\t"
    "v_mfma_f32_16x16x32_bf16 v[168:171], a[200:203], v[72:75], v[168:171]\n\t"
    "v_mfma_f32_16x16x32_bf16 v[172:175], a[200:203], v[76:79], v[172:175]\n\t"
    "v_mfma_f32_16x16x32_bf16 v[176:179], a[204:207], v[64:67], v[176:179]\n\t"
    "v_mfma_f32_16x16x32_bf16 v[180:183], a[204:207], v[68:71], v[180:183]\n\t"
    "v_mfma_f32_16x16x32_bf16 v[184:187], a[204:207], v[72:75], v[184:187]\n\t"
    "s_add_u32 m0, s56, 4096\n\t"
    "s_xor_b32 s80, s68, 8320\n\t"
    "buffer_load_dwordx4 v226, s[28:31], s80 offen nt lds\n\t"
    "v_mfma_f32_16x16x32_bf16 v[188:191], a[204:207], v[76:79], v[188:191]\n\t"
    "v_mfma_f32_16x16x32_bf16 v[128:131], a[208:211], v[80:83], v[128:131]\n\t"
    "v_mfma_f32_16x16x32_bf16 v[132:135], a[208:211], v[84:87], v[132:135]\n\t"
    "v_mfma_f32_16x16x32_bf16 v[136:139], a[208:211], v[88:91], v[136:139]\n\t"
    "v_mfma_f32_16x16x32_bf16 v[140:143], a[208:211], v[92:95], v[140:143]\n\t"
    "v_mfma_f32_16x16x32_bf16 v[144:147], a[212:215], v[80:83], v[144:147]\n\t"
    "buffer_load_dwordx4 v227, s[28:31], s80 offen offset:1024 nt lds\n\t"
    "v_mfma_f32_16x16x32_bf16 v[148:151], a[212:215], v[84:87], v[148:151]\n\t"
    "v_mfma_f32_16x16x32_bf16 v[152:155], a[212:215], v[88:91], v[152:155]\n\t"
    "v_mfma_f32_16x16x32_bf16 v[156:159], a[212:215], v[92:95], v[156:159]\n\t"
    "v_mfma_f32_16x16x32_bf16 v[160:163], a[216:219], v[80:83], v[160:163]\n\t"
    "v_mfma_f32_16x16x32_bf16 v[164:167], a[216:219], v[84:87], v[164:167]\n\t"
    "v_mfma_f32_16x16x32_bf16 v[168:171], a[216:219], v[88:91], v[168:171]\n\t"
    "buffer_load_dwordx4 v228, s[28:31], s80 offen offset:2048 nt lds\n\t"
    "v_mfma_f32_16x16x32_bf16 v[172:175], a[216:219], v[92:95], v[172:175]\n\t"
    "v_mfma_f32_16x16x32_bf16 v[176:179], a[220:223], v[80:83], v[176:179]\n\t"
    "v_mfma_f32_16x16x32_bf16 v[180:183], a[220:223], v[84:87], v[180:183]\n\t"
    "v_mfma_f32_16x16x32_bf16 v[184:187], a[220:223], v[88:91], v[184:187]\n\t"
    "v_mfma_f32_16x16x32_bf16 v[188:191], a[220:223], v[92:95], v[188:191]\n\t"
    "v_cvt_pk_bf16_f32 v0, v128, v129\n\t"
    "v_cvt_pk_bf16_f32 v1, v130, v131\n\t"
    "ds_write_b64 v206, v[0:1] offset:32768\n\t"
    "v_cvt_pk_bf16_f32 v2, v132, v133\n\t"
    "v_cvt_pk_bf16_f32 v3, v134, v135\n\t"
    "ds_write_b64 v206, v[2:3] offset:33024\n\t"
    "buffer_load_dwordx4 v229, s[28:31], s80 offen offset:3072 nt lds\n\t"
    "v_cvt_pk_bf16_f32 v4, v136, v137\n\t"
    "v_cvt_pk_bf16_f32 v5, v138, v139\n\t"
    "ds_write_b64 v206, v[4:5] offset:33792\n\t"
    "v_cvt_pk_bf16_f32 v6, v140, v141\n\t"
    "v_cvt_pk_bf16_f32 v7, v142, v143\n\t"
    "ds_write_b64 v206, v[6:7] offset:34048\n\t"
    "v_cvt_pk_bf16_f32 v8, v144, v145\n\t"
    "v_cvt_pk_bf16_f32 v9, v146, v147\n\t"
    "ds_write_b64 v206, v[8:9] offset:34816\n\t"
    "v_cvt_pk_bf16_f32 v10, v148, v149\n\t"
    "v_cvt_pk_bf16_f32 v11, v150, v151\n\t"
    "ds_write_b64 v206, v[10:11] offset:35072\n\t"
    "s_add_u32 m0, s56, 24576\n\t"
    "s_xor_b32 s80, s68, 8448\n\t"
    "buffer_load_dwordx4 v226, s[28:31], s80 offen nt lds\n\t"
    "v_cvt_pk_bf16_f32 v12, v152, v153\n\t"
    "v_cvt_pk_bf16_f32 v13, v154, v155\n\t"
    "ds_write_b64 v206, v[12:13] offset:35840\n\t"
    "v_cvt_pk_bf16_f32 v14, v156, v157\n\t"
    "v_cvt_pk_bf16_f32 v15, v158, v159\n\t"
    "ds_write_b64 v206, v[14:15] offset:36096\n\t"
    "v_cvt_pk_bf16_f32 v16, v160, v161\n\t"
    "v_cvt_pk_bf16_f32 v17, v162, v163\n\t"
    "ds_write_b64 v206, v[16:17] offset:36864\n\t"
    "v_cvt_pk_bf16_f32 v18, v164, v165\n\t"
    "v_cvt_pk_bf16_f32 v19, v166, v167\n\t"
    "ds_write_b64 v206, v[18:19] offset:37120\n\t"
    "buffer_load_dwordx4 v227, s[28:31], s80 offen offset:1024 nt lds\n\t"
    "v_cvt_pk_bf16_f32 v20, v168, v169\n\t"
    "v_cvt_pk_bf16_f32 v21, v170, v171\n\t"
    "ds_write_b64 v206, v[20:21] offset:37888\n\t"
    "v_cvt_pk_bf16_f32 v22, v172, v173\n\t"
    "v_cvt_pk_bf16_f32 v23, v174, v175\n\t"
    "ds_write_b64 v206, v[22:23] offset:38144\n\t"
    "v_cvt_pk_bf16_f32 v24, v176, v177\n\t"
    "v_cvt_pk_bf16_f32 v25, v178, v179\n\t"
    "ds_write_b64 v206, v[24:25] offset:38912\n\t"
    "v_cvt_pk_bf16_f32 v26, v180, v181\n\t"
    "v_cvt_pk_bf16_f32 v27, v182, v183\n\t"
    "ds_write_b64 v206, v[26:27] offset:39168\n\t"
    "buffer_load_dwordx4 v228, s[28:31], s80 offen offset:2048 nt lds\n\t"
    "v_cvt_pk_bf16_f32 v28, v184, v185\n\t"
    "v_cvt_pk_bf16_f32 v29, v186, v187\n\t"
    "ds_write_b64 v206, v[28:29] offset:39936\n\t"
    "v_cvt_pk_bf16_f32 v30, v188, v189\n\t"
    "v_cvt_pk_bf16_f32 v31, v190, v191\n\t"
    "ds_write_b64 v206, v[30:31] offset:40192\n\t"
    "s_waitcnt vmcnt(23)\n\t"
    "ds_read_b128 a[128:131], v230 offset:8192\n\t"
    "ds_read_b128 a[132:135], v231 offset:8192\n\t"
    "ds_read_b128 a[136:139], v232 offset:8192\n\t"
    "ds_read_b128 a[140:143], v233 offset:8192\n\t"
    "s_waitcnt lgkmcnt(0)\n\t"
    "s_barrier\n\t"
    "ds_read_b128 a[0:3], v215 offset:0\n\t"
    "ds_read_b128 a[4:7], v217 offset:0\n\t"
    "buffer_load_dwordx4 v229, s[28:31], s80 offen offset:3072 nt lds\n\t"
    "ds_read_b128 a[8:11], v215 offset:2048\n\t"
    "ds_read_b128 a[12:15], v217 offset:2048\n\t"
    "s_add_u32 m0, s56, 28672\n\t"
    "s_xor_b32 s80, s68, 8576\n\t"
    "buffer_load_dwordx4 v226, s[28:31], s80 offen nt lds\n\t"
    "ds_read_b128 a[16:19], v215 offset:4096\n\t"
    "ds_read_b128 a[20:23], v217 offset:4096\n\t"
    "buffer_load_dwordx4 v227, s[28:31], s80 offen offset:1024 nt lds\n\t"
    "ds_read_b128 a[24:27], v215 offset:6144\n\t"
    "ds_read_b128 a[28:31], v217 offset:6144\n\t"
    "ds_read_b128 a[32:35], v215 offset:40960\n\t"
    "ds_read_b128 a[36:39], v217 offset:40960\n\t"
    "ds_read_b128 a[40:43], v215 offset:43008\n\t"
    "ds_read_b128 a[44:47], v217 offset:43008\n\t"
    "ds_read_b128 a[48:51], v215 offset:45056\n\t"
    "ds_read_b128 a[52:55], v217 offset:45056\n\t"
    "ds_read_b128 a[56:59], v215 offset:47104\n\t"
    "ds_read_b128 a[60:63], v217 offset:47104\n\t"
    "ds_read_b128 a[64:67], v216 offset:0\n\t"
    "ds_read_b128 a[68:71], v218 offset:0\n\t"
    "buffer_load_dwordx4 v228, s[28:31], s80 offen offset:2048 nt lds\n\t"
    "ds_read_b128 a[72:75], v216 offset:2048\n\t"
    "ds_read_b128 a[76:79], v218 offset:2048\n\t"
    "ds_read_b128 a[80:83], v216 offset:4096\n\t"
    "ds_read_b128 a[84:87], v218 offset:4096\n\t"
    "ds_read_b128 a[88:91], v216 offset:6144\n\t"
    "ds_read_b128 a[92:95], v218 offset:6144\n\t"
    "ds_read_b128 a[96:99], v216 offset:40960\n\t"
    "ds_read_b128 a[100:103], v218 offset:40960\n\t"
    "ds_read_b128 a[104:107], v216 offset:43008\n\t"
    "ds_read_b128 a[108:111], v218 offset:43008\n\t"
    "ds_read_b128 a[112:115], v216 offset:45056\n\t"
    "ds_read_b128 a[116:119], v218 offset:45056\n\t"
    "ds_read_b128 a[120:123], v216 offset:47104\n\t"
    "ds_read_b128 a[124:127], v218 offset:47104\n\t"
    "buffer_load_dwordx4 v229, s[28:31], s80 offen offset:3072 nt lds\n\t"
    "s_waitcnt lgkmcnt(14)\n\t"
    "v_mfma_f32_32x32x16_bf16 v[128:143], a[128:131], a[0:3], 0\n\t"
    "s_add_u32 m0, s56, 8192\n\t"
    "s_xor_b32 s80, s68, 131072\n\t"
    "buffer_load_dwordx4 v226, s[28:31], s80 offen nt lds\n\t"
    "v_mfma_f32_32x32x16_bf16 v[144:159], a[128:131], a[4:7], 0\n\t"
    "v_mfma_f32_32x32x16_bf16 v[128:143], a[132:135], a[8:11], v[128:143]\n\t"
    "buffer_load_dwordx4 v227, s[28:31], s80 offen offset:1024 nt lds\n\t"
    "v_mfma_f32_32x32x16_bf16 v[144:159], a[132:135], a[12:15], v[144:159]\n\t"
    "s_waitcnt vmcnt(26)\n\t"
    "ds_read_b128 a[144:147], v230 offset:12288\n\t"
    "ds_read_b128 a[148:151], v231 offset:12288\n\t"
    "v_mfma_f32_32x32x16_bf16 v[128:143], a[136:139], a[16:19], v[128:143]\n\t"
    "ds_read_b128 a[152:155], v232 offset:12288\n\t"
    "ds_read_b128 a[156:159], v233 offset:12288\n\t"
    "v_mfma_f32_32x32x16_bf16 v[144:159], a[136:139], a[20:23], v[144:159]\n\t"
    "buffer_load_dwordx4 v228, s[28:31], s80 offen offset:2048 nt lds\n\t"
    "v_mfma_f32_32x32x16_bf16 v[128:143], a[140:143], a[24:27], v[128:143]\n\t"
    "v_mfma_f32_32x32x16_bf16 v[144:159], a[140:143], a[28:31], v[144:159]\n\t"
    "s_waitcnt lgkmcnt(0)\n\t"
    "v_mfma_f32_32x32x16_bf16 v[128:143], a[144:147], a[32:35], v[128:143]\n\t"
    "v_mfma_f32_32x32x16_bf16 v[144:159], a[144:147], a[36:39], v[144:159]\n\t"
    "v_mfma_f32_32x32x16_bf16 v[128:143], a[148:151], a[40:43], v[128:143]\n\t"
    "v_mfma_f32_32x32x16_bf16 v[144:159], a[148:151], a[44:47], v[144:159]\n\t"
    "s_waitcnt vmcnt(23)\n\t"
    "ds_read_b128 a[128:131], v230 offset:16384\n\t"
    "ds_read_b128 a[132:135], v231 offset:16384\n\t"
    "v_mfma_f32_32x32x16_bf16 v[128:143], a[152:155], a[48:51], v[128:143]\n\t"
    "ds_read_b128 a[136:139], v232 offset:16384\n\t"
    "ds_read_b128 a[140:143], v233 offset:16384\n\t"
    "v_mfma_f32_32x32x16_bf16 v[144:159], a[152:155], a[52:55], v[144:159]\n\t"
    "buffer_load_dwordx4 v229, s[28:31], s80 offen offset:3072 nt lds\n\t"
    "v_mfma_f32_32x32x16_bf16 v[128:143], a[156:159], a[56:59], v[128:143]\n\t"
    "v_mfma_f32_32x32x16_bf16 v[144:159], a[156:159], a[60:63], v[144:159]\n\t"
    "s_waitcnt lgkmcnt(0)\n\t"
    "s_barrier\n\t"
    "v_mfma_f32_32x32x16_bf16 v[128:143], a[128:131], a[64:67], v[128:143]\n\t"
    "s_add_u32 m0, s56, 12288\n\t"
    "s_xor_b32 s80, s68, 131200\n\t"
    "buffer_load_dwordx4 v226, s[28:31], s80 offen nt lds\n\t"
    "v_mfma_f32_32x32x16_bf16 v[144:159], a[128:131], a[68:71], v[144:159]\n\t"
    "buffer_load_dwordx4 v227, s[28:31], s80 offen offset:1024 nt lds\n\t"
    "v_mfma_f32_32x32x16_bf16 v[128:143], a[132:135], a[72:75], v[128:143]\n\t"
    "v_mfma_f32_32x32x16_bf16 v[144:159], a[132:135], a[76:79], v[144:159]\n\t"
    "s_waitcnt vmcnt(22)\n\t"
    "ds_read_b128 a[144:147], v230 offset:20480\n\t"
    "ds_read_b128 a[148:151], v231 offset:20480\n\t"
    "v_mfma_f32_32x32x16_bf16 v[128:143], a[136:139], a[80:83], v[128:143]\n\t"
    "buffer_load_dwordx4 v228, s[28:31], s80 offen offset:2048 nt lds\n\t"
    "ds_read_b128 a[152:155], v232 offset:20480\n\t"
    "ds_read_b128 a[156:159], v233 offset:20480\n\t"
    "v_mfma_f32_32x32x16_bf16 v[144:159], a[136:139], a[84:87], v[144:159]\n\t"
    "v_mfma_f32_32x32x16_bf16 v[128:143], a[140:143], a[88:91], v[128:143]\n\t"
    "v_mfma_f32_32x32x16_bf16 v[144:159], a[140:143], a[92:95], v[144:159]\n\t"
    "buffer_load_dwordx4 v229, s[28:31], s80 offen offset:3072 nt lds\n\t"
    "s_waitcnt lgkmcnt(0)\n\t"
    "v_mfma_f32_32x32x16_bf16 v[128:143], a[144:147], a[96:99], v[128:143]\n\t"
    "v_mfma_f32_32x32x16_bf16 v[144:159], a[144:147], a[100:103], v[144:159]\n\t"
    "s_add_u32 m0, s56, 16384\n\t"
    "s_xor_b32 s80, s68, 131328\n\t"
    "buffer_load_dwordx4 v226, s[28:31], s80 offen nt lds\n\t"
    "v_mfma_f32_32x32x16_bf16 v[128:143], a[148:151], a[104:107], v[128:143]\n\t"
    "v_mfma_f32_32x32x16_bf16 v[144:159], a[148:151], a[108:111], v[144:159]\n\t"
    "s_waitcnt vmcnt(21)\n\t"
    "ds_read_b128 a[128:131], v230 offset:0\n\t"
    "ds_read_b128 a[132:135], v231 offset:0\n\t"
    "v_mfma_f32_32x32x16_bf16 v[128:143], a[152:155], a[112:115], v[128:143]\n\t"
    "buffer_load_dwordx4 v227, s[28:31], s80 offen offset:1024 nt lds\n\t"
    "ds_read_b128 a[136:139], v232 offset:0\n\t"
    "ds_read_b128 a[140:143], v233 offset:0\n\t"
    "v_mfma_f32_32x32x16_bf16 v[144:159], a[152:155], a[116:119], v[144:159]\n\t"
    "v_mfma_f32_32x32x16_bf16 v[128:143], a[156:159], a[120:123], v[128:143]\n\t"
    "v_mfma_f32_32x32x16_bf16 v[144:159], a[156:159], a[124:127], v[144:159]\n\t"
    "buffer_load_dwordx4 v228, s[28:31], s80 offen offset:2048 nt lds\n\t"
    "s_waitcnt lgkmcnt(0)\n\t"
    "v_mfma_f32_32x32x16_bf16 v[160:175], a[128:131], a[0:3], 0\n\t"
    "v_mfma_f32_32x32x16_bf16 v[176:191], a[128:131], a[4:7], 0\n\t"
    "v_mfma_f32_32x32x16_bf16 v[160:175], a[132:135], a[8:11], v[160:175]\n\t"
    "s_nop 5\n\t"
    "v_max_f32 v208, v128, v129\n\t"
    "v_max_f32 v209, v144, v145\n\t"
    "v_max3_f32 v208, v208, v130, v131\n\t"
    "v_max3_f32 v209, v209, v146, v147\n\t"
    "v_max3_f32 v208, v208, v132, v133\n\t"
    "v_mfma_f32_32x32x16_bf16 v[176:191], a[132:135], a[12:15], v[176:191]\n\t"
    "v_max3_f32 v209, v209, v148, v149\n\t"
    "v_max3_f32 v208, v208, v134, v135\n\t"
    "v_max3_f32 v209, v209, v150, v151\n\t"
    "v_max3_f32 v208, v208, v136, v137\n\t"
    "v_max3_f32 v209, v209, v152, v153\n\t"
    "v_max3_f32 v208, v208, v138, v139\n\t"
    "s_waitcnt vmcnt(19)\n\t"
    "ds_read_b128 a[144:147], v230 offset:4096\n\t"
    "ds_read_b128 a[148:151], v231 offset:4096\n\t"
    "v_mfma_f32_32x32x16_bf16 v[160:175], a[136:139], a[16:19], v[160:175]\n\t"
    "v_max3_f32 v209, v209, v154, v155\n\t"
    "v_max3_f32 v208, v208, v140, v141\n\t"
    "v_max3_f32 v209, v209, v156, v157\n\t"
    "v_max3_f32 v208, v208, v142, v143\n\t"
    "v_max3_f32 v209, v209, v158, v159\n\t"
    "ds_read_b128 a[152:155], v232 offset:4096\n\t"
    "ds_read_b128 a[156:159], v233 offset:4096\n\t"
    "v_mfma_f32_32x32x16_bf16 v[176:191], a[136:139], a[20:23], v[176:191]\n\t"
    "buffer_load_dwordx4 v229, s[28:31], s80 offen offset:3072 nt lds\n\t"
    "v_mov_b32 v218, v208\n\t"
    "v_mov_b32 v219, v209\n\t"
    "s_nop 0\n\t"
    "v_permlane32_swap_b32 v218, v208\n\t"
    "v_permlane32_swap_b32 v219, v209\n\t"
    "v_max_f32 v208, v208, v218\n\t"
    "v_max_f32 v209, v209, v219\n\t"
    "v_mfma_f32_32x32x16_bf16 v[160:175], a[140:143], a[24:27], v[160:175]\n\t"
    "ds_write_b64 v224, v[208:209] offset:36864\n\t"
    "v_accvgpr_read_b32 v214, a254\n\t"
    "v_accvgpr_read_b32 v215, a255\n\t"
    "v_mfma_f32_32x32x16_bf16 v[176:191], a[140:143], a[28:31], v[176:191]\n\t"
    "s_add_u32 m0, s56, 20480\n\t"
    "s_xor_b32 s80, s68, 131456\n\t"
    "buffer_load_dwordx4 v226, s[28:31], s80 offen nt lds\n\t"
    "s_waitcnt lgkmcnt(0)\n\t"
    "s_barrier\n\t"
    "ds_read_b64 v[192:193], v251 offset:36864\n\t"
    "ds_read_b64 v[194:195], v252 offset:36864\n\t"
    "ds_read_b64 v[196:197], v253 offset:36864\n\t"
    "v_mfma_f32_32x32x16_bf16 v[160:175], a[144:147], a[32:35], v[160:175]\n\t"
    "buffer_load_dwordx4 v227, s[28:31], s80 offen offset:1024 nt lds\n\t"
    "v_mfma_f32_32x32x16_bf16 v[176:191], a[144:147], a[36:39], v[176:191]\n\t"
    "buffer_load_dwordx4 v228, s[28:31], s80 offen offset:2048 nt lds\n\t"
    "s_waitcnt lgkmcnt(2)\n\t"
    "v_max3_f32 v214, v214, v208, v193\n\t"
    "v_mfma_f32_32x32x16_bf16 v[160:175], a[148:151], a[40:43], v[160:175]\n\t"
    "buffer_load_dwordx4 v229, s[28:31], s80 offen offset:3072 nt lds\n\t"
    "v_max3_f32 v215, v215, v209, v192\n\t"
    "s_waitcnt lgkmcnt(0)\n\t"
    "v_max3_f32 v214, v214, v194, v197\n\t"
    "v_max3_f32 v215, v215, v195, v196\n\t"
    "v_mul_f32 v214, -s69, v214\n\t"
    "v_mul_f32 v215, -s69, v215\n\t"
    "v_mfma_f32_32x32x16_bf16 v[176:191], a[148:151], a[44:47], v[176:191]\n\t"
    "v_fma_f32 v128, v128, s69, v214\n\t"
    "v_fma_f32 v129, v129, s69, v214\n\t"
    "v_fma_f32 v144, v144, s69, v215\n\t"
    "v_fma_f32 v145, v145, s69, v215\n\t"
    "v_exp_f32 v128, v128\n\t"
    "v_exp_f32 v129, v129\n\t"
    "s_waitcnt vmcnt(20)\n\t"
    "ds_read_b128 a[128:131], v230 offset:24576\n\t"
    "ds_read_b128 a[132:135], v231 offset:24576\n\t"
    "v_mfma_f32_32x32x16_bf16 v[160:175], a[152:155], a[48:51], v[160:175]\n\t"
    "s_add_u32 m0, s56, 0\n\t"
    "s_xor_b32 s80, s68, 139264\n\t"
    "buffer_load_dwordx4 v226, s[28:31], s80 offen nt lds\n\t"
    "v_exp_f32 v144, v144\n\t"
    "v_exp_f32 v145, v145\n\t"
    "v_cvt_pk_bf16_f32 v0, v128, v129\n\t"
    "v_mov_b32 v210, v128\n\t"
    "v_mov_b32 v212, v129\n\t"
    "ds_read_b128 a[136:139], v232 offset:24576\n\t"
    "ds_read_b128 a[140:143], v233 offset:24576\n\t"
    "v_mfma_f32_32x32x16_bf16 v[176:191], a[152:155], a[52:55], v[176:191]\n\t"
    "buffer_load_dwordx4 v227, s[28:31], s80 offen offset:1024 nt lds\n\t"
    "v_cvt_pk_bf16_f32 v192, v144, v145\n\t"
    "v_mov_b32 v211, v144\n\t"
    "v_mov_b32 v213, v145\n\t"
    "v_fma_f32 v130, v130, s69, v214\n\t"
    "v_fma_f32 v131, v131, s69, v214\n\t"
    "v_fma_f32 v146, v146, s69, v215\n\t"
    "v_mfma_f32_32x32x16_bf16 v[160:175], a[156:159], a[56:59], v[160:175]\n\t"
    "v_fma_f32 v147, v147, s69, v215\n\t"
    "v_exp_f32 v130, v130\n\t"
    "v_exp_f32 v131, v131\n\t"
    "v_exp_f32 v146, v146\n\t"
    "v_exp_f32 v147, v147\n\t"
    "v_mfma_f32_32x32x16_bf16 v[176:191], a[156:159], a[60:63], v[176:191]\n\t"
    "buffer_load_dwordx4 v228, s[28:31], s80 offen offset:2048 nt lds\n\t"
    "v_cvt_pk_bf16_f32 v1, v130, v131\n\t"
    "v_add_f32 v210, v210, v130\n\t"
    "v_add_f32 v212, v212, v131\n\t"
    "v_cvt_pk_bf16_f32 v193, v146, v147\n\t"
    "v_add_f32 v211, v211, v146\n\t"
    "v_add_f32 v213, v213, v147\n\t"
    "s_waitcnt lgkmcnt(0)\n\t"
    "v_mfma_f32_32x32x16_bf16 v[160:175], a[128:131], a[64:67], v[160:175]\n\t"
    "v_fma_f32 v132, v132, s69, v214\n\t"
    "v_fma_f32 v133, v133, s69, v214\n\t"
    "v_fma_f32 v148, v148, s69, v215\n\t"
    "v_fma_f32 v149, v149, s69, v215\n\t"
    "v_exp_f32 v132, v132\n\t"
    "v_mfma_f32_32x32x16_bf16 v[176:191], a[128:131], a[68:71], v[176:191]\n\t"
    "v_exp_f32 v133, v133\n\t"
    "v_exp_f32 v148, v148\n\t"
    "v_exp_f32 v149, v149\n\t"
    "v_cvt_pk_bf16_f32 v2, v132, v133\n\t"
    "v_add_f32 v210, v210, v132\n\t"
    "v_add_f32 v212, v212, v133\n\t"
    "v_mfma_f32_32x32x16_bf16 v[160:175], a[132:135], a[72:75], v[160:175]\n\t"
    "v_cvt_pk_bf16_f32 v194, v148, v149\n\t"
    "v_add_f32 v211, v211, v148\n\t"
    "v_add_f32 v213, v213, v149\n\t"
    "v_fma_f32 v134, v134, s69, v214\n\t"
    "v_fma_f32 v135, v135, s69, v214\n\t"
    "v_mfma_f32_32x32x16_bf16 v[176:191], a[132:135], a[76:79], v[176:191]\n\t"
    "v_fma_f32 v150, v150, s69, v215\n\t"
    "v_fma_f32 v151, v151, s69, v215\n\t"
    "v_exp_f32 v134, v134\n\t"
    "v_exp_f32 v135, v135\n\t"
    "v_exp_f32 v150, v150\n\t"
    "v_exp_f32 v151, v151\n\t"
    "s_waitcnt vmcnt(19)\n\t"
    "ds_read_b128 a[144:147], v230 offset:28672\n\t"
    "ds_read_b128 a[148:151], v231 offset:28672\n\t"
    "v_mfma_f32_32x32x16_bf16 v[160:175], a[136:139], a[80:83], v[160:175]\n\t"
    "v_cvt_pk_bf16_f32 v3, v134, v135\n\t"
    "v_add_f32 v210, v210, v134\n\t"
    "v_add_f32 v212, v212, v135\n\t"
    "ds_write_b128 v224, v[0:3] offset:32768\n\t"
    "v_cvt_pk_bf16_f32 v195, v150, v151\n\t"
    "ds_read_b128 a[152:155], v232 offset:28672\n\t"
    "ds_read_b128 a[156:159], v233 offset:28672\n\t"
    "v_mfma_f32_32x32x16_bf16 v[176:191], a[136:139], a[84:87], v[176:191]\n\t"
    "buffer_load_dwordx4 v229, s[28:31], s80 offen offset:3072 nt lds\n\t"
    "v_add_f32 v211, v211, v150\n\t"
    "v_add_f32 v213, v213, v151\n\t"
    "ds_write_b128 v224, v[192:195] offset:34816\n\t"
    "v_fma_f32 v136, v136, s69, v214\n\t"
    "v_fma_f32 v137, v137, s69, v214\n\t"
    "v_fma_f32 v152, v152, s69, v215\n\t"
    "v_mfma_f32_32x32x16_bf16 v[160:175], a[140:143], a[88:91], v[160:175]\n\t"
    "v_fma_f32 v153, v153, s69, v215\n\t"
    "v_exp_f32 v136, v136\n\t"
    "v_exp_f32 v137, v137\n\t"
    "v_exp_f32 v152, v152\n\t"
    "v_exp_f32 v153, v153\n\t"
    "v_mfma_f32_32x32x16_bf16 v[176:191], a[140:143], a[92:95], v[176:191]\n\t"
    "s_add_u32 m0, s56, 4096\n\t"
    "s_xor_b32 s80, s68, 139392\n\t"
    "buffer_load_dwordx4 v226, s[28:31], s80 offen nt lds\n\t"
    "v_cvt_pk_bf16_f32 v4, v136, v137\n\t"
    "v_add_f32 v210, v210, v136\n\t"
    "v_add_f32 v212, v212, v137\n\t"
    "v_cvt_pk_bf16_f32 v196, v152, v153\n\t"
    "v_add_f32 v211, v211, v152\n\t"
    "v_add_f32 v213, v213, v153\n\t"
    "s_waitcnt lgkmcnt(1)\n\t"
    "v_mfma_f32_32x32x16_bf16 v[160:175], a[144:147], a[96:99], v[160:175]\n\t"
    "v_fma_f32 v138, v138, s69, v214\n\t"
    "v_fma_f32 v139, v139, s69, v214\n\t"
    "v_fma_f32 v154, v154, s69, v215\n\t"
    "v_fma_f32 v155, v155, s69, v215\n\t"
    "v_exp_f32 v138, v138\n\t"
    "v_mfma_f32_32x32x16_bf16 v[176:191], a[144:147], a[100:103], v[176:191]\n\t"
    "buffer_load_dwordx4 v227, s[28:31], s80 offen offset:1024 nt lds\n\t"
    "v_exp_f32 v139, v139\n\t"
    "v_exp_f32 v154, v154\n\t"
    "v_exp_f32 v155, v155\n\t"
    "v_cvt_pk_bf16_f32 v5, v138, v139\n\t"
    "v_add_f32 v210, v210, v138\n\t"
    "v_add_f32 v212, v212, v139\n\t"
    "v_mfma_f32_32x32x16_bf16 v[160:175], a[148:151], a[104:107], v[160:175]\n\t"
    "v_cvt_pk_bf16_f32 v197, v154, v155\n\t"
    "v_add_f32 v211, v211, v154\n\t"
    "v_add_f32 v213, v213, v155\n\t"
    "v_fma_f32 v140, v140, s69, v214\n\t"
    "v_fma_f32 v141, v141, s69, v214\n\t"
    "v_mfma_f32_32x32x16_bf16 v[176:191], a[148:151], a[108:111], v[176:191]\n\t"
    "buffer_load_dwordx4 v228, s[28:31], s80 offen offset:2048 nt lds\n\t"
    "v_fma_f32 v156, v156, s69, v215\n\t"
    "v_fma_f32 v157, v157, s69, v215\n\t"
    "v_exp_f32 v140, v140\n\t"
    "v_exp_f32 v141, v141\n\t"
    "v_exp_f32 v156, v156\n\t"
    "v_exp_f32 v157, v157\n\t"
    "s_waitcnt vmcnt(19)\n\t"
    "ds_read_b128 a[128:131], v230 offset:8192\n\t"
    "ds_read_b128 a[132:135], v231 offset:8192\n\t"
    "v_mfma_f32_32x32x16_bf16 v[160:175], a[152:155], a[112:115], v[160:175]\n\t"
    "v_cvt_pk_bf16_f32 v6, v140, v141\n\t"
    "v_add_f32 v210, v210, v140\n\t"
    "v_add_f32 v212, v212, v141\n\t"
    "v_cvt_pk_bf16_f32 v198, v156, v157\n\t"
    "v_add_f32 v211, v211, v156\n\t"
    "ds_read_b128 a[136:139], v232 offset:8192\n\t"
    "ds_read_b128 a[140:143], v233 offset:8192\n\t"
    "v_mfma_f32_32x32x16_bf16 v[176:191], a[152:155], a[116:119], v[176:191]\n\t"
    "buffer_load_dwordx4 v229, s[28:31], s80 offen offset:3072 nt lds\n\t"
    "v_add_f32 v213, v213, v157\n\t"
    "v_fma_f32 v142, v142, s69, v214\n\t"
    "v_fma_f32 v143, v143, s69, v214\n\t"
    "v_fma_f32 v158, v158, s69, v215\n\t"
    "v_fma_f32 v159, v159, s69, v215\n\t"
    "v_exp_f32 v142, v142\n\t"
    "v_mfma_f32_32x32x16_bf16 v[160:175], a[156:159], a[120:123], v[160:175]\n\t"
    "v_exp_f32 v143, v143\n\t"
    "v_exp_f32 v158, v158\n\t"
    "v_exp_f32 v159, v159\n\t"
    "v_cvt_pk_bf16_f32 v7, v142, v143\n\t"
    "v_add_f32 v210, v210, v142\n\t"
    "v_mfma_f32_32x32x16_bf16 v[176:191], a[156:159], a[124:127], v[176:191]\n\t"
    "s_add_u32 m0, s56, 24576\n\t"
    "s_xor_b32 s80, s68, 139520\n\t"
    "buffer_load_dwordx4 v226, s[28:31], s80 offen nt lds\n\t"
    "v_add_f32 v212, v212, v143\n\t"
    "ds_write_b128 v224, v[4:7] offset:33792\n\t"
    "v_cvt_pk_bf16_f32 v199, v158, v159\n\t"
    "v_add_f32 v211, v211, v158\n\t"
    "v_add_f32 v213, v213, v159\n\t"
    "ds_write_b128 v224, v[196:199] offset:35840\n\t"
    "s_waitcnt lgkmcnt(2)\n\t"
    "s_waitcnt lgkmcnt(0)\n\t"
    "s_barrier\n\t"
    "v_mfma_f32_32x32x16_bf16 v[128:143], a[128:131], a[0:3], 0\n\t"
    "buffer_load_dwordx4 v227, s[28:31], s80 offen offset:1024 nt lds\n\t"
    "ds_read_b128 v[8:11], v251 offset:34816\n\t"
    "ds_read_b128 v[16:19], v252 offset:32768\n\t"
    "v_mfma_f32_32x32x16_bf16 v[144:159], a[128:131], a[4:7], 0\n\t"
    "buffer_load_dwordx4 v228, s[28:31], s80 offen offset:2048 nt lds\n\t"
    "ds_read_b128 v[24:27], v253 offset:34816\n\t"
    "ds_read_b128 v[12:15], v251 offset:35840\n\t"
    "v_mfma_f32_32x32x16_bf16 v[128:143], a[132:135], a[8:11], v[128:143]\n\t"
    "v_max3_f32 v208, v208, v160, v161\n\t"
    "v_max3_f32 v209, v209, v176, v177\n\t"
    "v_fma_f32 v160, v160, s69, v214\n\t"
    "v_fma_f32 v161, v161, s69, v214\n\t"
    "v_mfma_f32_32x32x16_bf16 v[144:159], a[132:135], a[12:15], v[144:159]\n\t"
    "v_fma_f32 v176, v176, s69, v215\n\t"
    "v_fma_f32 v177, v177, s69, v215\n\t"
    "v_exp_f32 v160, v160\n\t"
    "v_exp_f32 v161, v161\n\t"
    "s_waitcnt vmcnt(19)\n\t"
    "ds_read_b128 a[144:147], v230 offset:12288\n\t"
    "ds_read_b128 a[148:151], v231 offset:12288\n\t"
    "v_mfma_f32_32x32x16_bf16 v[128:143], a[136:139], a[16:19], v[128:143]\n\t"
    "v_exp_f32 v176, v176\n\t"
    "v_exp_f32 v177, v177\n\t"
    "v_cvt_pk_bf16_f32 v32, v160, v161\n\t"
    "v_add_f32 v210, v210, v160\n\t"
    "v_add_f32 v212, v212, v161\n\t"
    "ds_read_b128 a[152:155], v232 offset:12288\n\t"
    "ds_read_b128 a[156:159], v233 offset:12288\n\t"
    "v_mfma_f32_32x32x16_bf16 v[144:159], a[136:139], a[20:23], v[144:159]\n\t"
    "buffer_load_dwordx4 v229, s[28:31], s80 offen offset:3072 nt lds\n\t"
    "ds_read_b128 v[20:23], v252 offset:33792\n\t"
    "ds_read_b128 v[28:31], v253 offset:35840\n\t"
    "v_cvt_pk_bf16_f32 v192, v176, v177\n\t"
    "v_add_f32 v211, v211, v176\n\t"
    "v_add_f32 v213, v213, v177\n\t"
    "v_max3_f32 v208, v208, v162, v163\n\t"
    "v_mfma_f32_32x32x16_bf16 v[128:143], a[140:143], a[24:27], v[128:143]\n\t"
    "v_max3_f32 v209, v209, v178, v179\n\t"
    "v_fma_f32 v162, v162, s69, v214\n\t"
    "v_fma_f32 v163, v163, s69, v214\n\t"
    "v_fma_f32 v178, v178, s69, v215\n\t"
    "v_fma_f32 v179, v179, s69, v215\n\t"
    "v_mfma_f32_32x32x16_bf16 v[144:159], a[140:143], a[28:31], v[144:159]\n\t"
    "s_add_u32 m0, s56, 28672\n\t"
    "s_xor_b32 s80, s68, 139648\n\t"
    "buffer_load_dwordx4 v226, s[28:31], s80 offen nt lds\n\t"
    "v_exp_f32 v162, v162\n\t"
    "v_exp_f32 v163, v163\n\t"
    "v_exp_f32 v178, v178\n\t"
    "v_exp_f32 v179, v179\n\t"
    "s_waitcnt lgkmcnt(2)\n\t"
    "v_mfma_f32_32x32x16_bf16 v[128:143], a[144:147], a[32:35], v[128:143]\n\t"
    "v_cvt_pk_bf16_f32 v33, v162, v163\n\t"
    "v_add_f32 v210, v210, v162\n\t"
    "v_add_f32 v212, v212, v163\n\t"
    "v_cvt_pk_bf16_f32 v193, v178, v179\n\t"
    "v_mfma_f32_32x32x16_bf16 v[144:159], a[144:147], a[36:39], v[144:159]\n\t"
    "buffer_load_dwordx4 v227, s[28:31], s80 offen offset:1024 nt lds\n\t"
    "v_add_f32 v211, v211, v178\n\t"
    "v_add_f32 v213, v213, v179\n\t"
    "v_max3_f32 v208, v208, v164, v165\n\t"
    "v_max3_f32 v209, v209, v180, v181\n\t"
    "v_fma_f32 v164, v164, s69, v214\n\t"
    "v_mfma_f32_32x32x16_bf16 v[128:143], a[148:151], a[40:43], v[128:143]\n\t"
    "v_fma_f32 v165, v165, s69, v214\n\t"
    "v_fma_f32 v180, v180, s69, v215\n\t"
    "v_fma_f32 v181, v181, s69, v215\n\t"
    "v_exp_f32 v164, v164\n\t"
    "v_mfma_f32_32x32x16_bf16 v[144:159], a[148:151], a[44:47], v[144:159]\n\t"
    "v_exp_f32 v165, v165\n\t"
    "v_exp_f32 v180, v180\n\t"
    "v_exp_f32 v181, v181\n\t"
    "v_cvt_pk_bf16_f32 v34, v164, v165\n\t"
    "v_add_f32 v210, v210, v164\n\t"
    "s_waitcnt vmcnt(18)\n\t"
    "ds_read_b128 a[128:131], v230 offset:16384\n\t"
    "ds_read_b128 a[132:135], v231 offset:16384\n\t"
    "v_mfma_f32_32x32x16_bf16 v[128:143], a[152:155], a[48:51], v[128:143]\n\t"
    "buffer_load_dwordx4 v228, s[28:31], s80 offen offset:2048 nt lds\n\t"
    "v_add_f32 v212, v212, v165\n\t"
    "v_cvt_pk_bf16_f32 v194, v180, v181\n\t"
    "v_add_f32 v211, v211, v180\n\t"
    "v_add_f32 v213, v213, v181\n\t"
    "ds_read_b128 a[136:139], v232 offset:16384\n\t"
    "ds_read_b128 a[140:143], v233 offset:16384\n\t"
    "v_mfma_f32_32x32x16_bf16 v[144:159], a[152:155], a[52:55], v[144:159]\n\t"
    "v_max3_f32 v208, v208, v166, v167\n\t"
    "v_max3_f32 v209, v209, v182, v183\n\t"
    "v_fma_f32 v166, v166, s69, v214\n\t"
    "v_fma_f32 v167, v167, s69, v214\n\t"
    "v_mfma_f32_32x32x16_bf16 v[128:143], a[156:159], a[56:59], v[128:143]\n\t"
    "buffer_load_dwordx4 v229, s[28:31], s80 offen offset:3072 nt lds\n\t"
    "v_fma_f32 v182, v182, s69, v215\n\t"
    "v_fma_f32 v183, v183, s69, v215\n\t"
    "v_exp_f32 v166, v166\n\t"
    "v_exp_f32 v167, v167\n\t"
    "v_exp_f32 v182, v182\n\t"
    "v_mfma_f32_32x32x16_bf16 v[144:159], a[156:159], a[60:63], v[144:159]\n\t"
    "v_exp_f32 v183, v183\n\t"
    "v_cvt_pk_bf16_f32 v35, v166, v167\n\t"
    "v_add_f32 v210, v210, v166\n\t"
    "v_add_f32 v212, v212, v167\n\t"
    "s_waitcnt lgkmcnt(0)\n\t"
    "v_mfma_f32_32x32x16_bf16 v[128:143], a[128:131], a[64:67], v[128:143]\n\t"
    "s_add_u32 m0, s56, 8192\n\t"
    "s_xor_b32 s80, s68, 16384\n\t"
    "buffer_load_dwordx4 v226, s[28:31], s80 offen nt lds\n\t"
    "ds_write_b128 v224, v[32:35] offset:36864\n\t"
    "v_cvt_pk_bf16_f32 v195, v182, v183\n\t"
    "v_add_f32 v211, v211, v182\n\t"
    "v_add_f32 v213, v213, v183\n\t"
    "ds_write_b128 v224, v[192:195] offset:38912\n\t"
    "v_mfma_f32_32x32x16_bf16 v[144:159], a[128:131], a[68:71], v[144:159]\n\t"
    "v_max3_f32 v208, v208, v168, v169\n\t"
    "v_max3_f32 v209, v209, v184, v185\n\t"
    "v_fma_f32 v168, v168, s69, v214\n\t"
    "v_fma_f32 v169, v169, s69, v214\n\t"
    "v_mfma_f32_32x32x16_bf16 v[128:143], a[132:135], a[72:75], v[128:143]\n\t"
    "buffer_load_dwordx4 v227, s[28:31], s80 offen offset:1024 nt lds\n\t"
    "v_fma_f32 v184, v184, s69, v215\n\t"
    "v_fma_f32 v185, v185, s69, v215\n\t"
    "v_exp_f32 v168, v168\n\t"
    "v_exp_f32 v169, v169\n\t"
    "v_mfma_f32_32x32x16_bf16 v[144:159], a[132:135], a[76:79], v[144:159]\n\t"
    "v_exp_f32 v184, v184\n\t"
    "v_exp_f32 v185, v185\n\t"
    "v_cvt_pk_bf16_f32 v36, v168, v169\n\t"
    "v_add_f32 v210, v210, v168\n\t"
    "v_add_f32 v212, v212, v169\n\t"
    "s_waitcnt vmcnt(18)\n\t"
    "ds_read_b128 a[144:147], v230 offset:20480\n\t"
    "ds_read_b128 a[148:151], v231 offset:20480\n\t"
    "v_mfma_f32_32x32x16_bf16 v[128:143], a[136:139], a[80:83], v[128:143]\n\t"
    "buffer_load_dwordx4 v228, s[28:31], s80 offen offset:2048 nt lds\n\t"
    "v_cvt_pk_bf16_f32 v196, v184, v185\n\t"
    "v_add_f32 v211, v211, v184\n\t"
    "v_add_f32 v213, v213, v185\n\t"
    "v_max3_f32 v208, v208, v170, v171\n\t"
    "ds_read_b128 a[152:155], v232 offset:20480\n\t"
    "ds_read_b128 a[156:159], v233 offset:20480\n\t"
    "v_mfma_f32_32x32x16_bf16 v[144:159], a[136:139], a[84:87], v[144:159]\n\t"
    "v_max3_f32 v209, v209, v186, v187\n\t"
    "v_fma_f32 v170, v170, s69, v214\n\t"
    "v_fma_f32 v171, v171, s69, v214\n\t"
    "v_fma_f32 v186, v186, s69, v215\n\t"
    "v_fma_f32 v187, v187, s69, v215\n\t"
    "v_mfma_f32_32x32x16_bf16 v[128:143], a[140:143], a[88:91], v[128:143]\n\t"
    "v_exp_f32 v170, v170\n\t"
    "v_exp_f32 v171, v171\n\t"
    "v_exp_f32 v186, v186\n\t"
    "v_exp_f32 v187, v187\n\t"
    "v_mfma_f32_32x32x16_bf16 v[144:159], a[140:143], a[92:95], v[144:159]\n\t"
    "v_cvt_pk_bf16_f32 v37, v170, v171\n\t"
    "v_add_f32 v210, v210, v170\n\t"
    "v_add_f32 v212, v212, v171\n\t"
    "v_cvt_pk_bf16_f32 v197, v186, v187\n\t"
    "s_waitcnt lgkmcnt(0)\n\t"
    "v_mfma_f32_32x32x16_bf16 v[128:143], a[144:147], a[96:99], v[128:143]\n\t"
    "v_add_f32 v211, v211, v186\n\t"
    "v_add_f32 v213, v213, v187\n\t"
    "v_max3_f32 v208, v208, v172, v173\n\t"
    "v_max3_f32 v209, v209, v188, v189\n\t"
    "v_fma_f32 v172, v172, s69, v214\n\t"
    "v_mfma_f32_32x32x16_bf16 v[144:159], a[144:147], a[100:103], v[144:159]\n\t"
    "v_fma_f32 v173, v173, s69, v214\n\t"
    "v_fma_f32 v188, v188, s69, v215\n\t"
    "v_fma_f32 v189, v189, s69, v215\n\t"
    "v_exp_f32 v172, v172\n\t"
    "v_mfma_f32_32x32x16_bf16 v[128:143], a[148:151], a[104:107], v[128:143]\n\t"
    "buffer_load_dwordx4 v229, s[28:31], s80 offen offset:3072 nt lds\n\t"
    "v_exp_f32 v173, v173\n\t"
    "v_exp_f32 v188, v188\n\t"
    "v_exp_f32 v189, v189\n\t"
    "v_cvt_pk_bf16_f32 v38, v172, v173\n\t"
    "v_add_f32 v210, v210, v172\n\t"
    "v_mfma_f32_32x32x16_bf16 v[144:159], a[148:151], a[108:111], v[144:159]\n\t"
    "v_add_f32 v212, v212, v173\n\t"
    "v_cvt_pk_bf16_f32 v198, v188, v189\n\t"
    "v_add_f32 v211, v211, v188\n\t"
    "v_add_f32 v213, v213, v189\n\t"
    "s_waitcnt vmcnt(16)\n\t"
    "ds_read_b128 a[128:131], v230 offset:0\n\t"
    "ds_read_b128 a[132:135], v231 offset:0\n\t"
    "v_mfma_f32_32x32x16_bf16 v[128:143], a[152:155], a[112:115], v[128:143]\n\t"
    "s_add_u32 m0, s56, 12288\n\t"
    "s_xor_b32 s80, s68, 16512\n\t"
    "buffer_load_dwordx4 v226, s[28:31], s80 offen nt lds\n\t"
    "v_max3_f32 v208, v208, v174, v175\n\t"
    "v_max3_f32 v209, v209, v190, v191\n\t"
    "v_fma_f32 v174, v174, s69, v214\n\t"
    "v_fma_f32 v175, v175, s69, v214\n\t"
    "ds_read_b128 a[136:139], v232 offset:0\n\t"
    "ds_read_b128 a[140:143], v233 offset:0\n\t"
    "v_mfma_f32_32x32x16_bf16 v[144:159], a[152:155], a[116:119], v[144:159]\n\t"
    "v_fma_f32 v190, v190, s69, v215\n\t"
    "v_fma_f32 v191, v191, s69, v215\n\t"
    "v_exp_f32 v174, v174\n\t"
    "v_exp_f32 v175, v175\n\t"
    "v_exp_f32 v190, v190\n\t"
    "v_mfma_f32_32x32x16_bf16 v[128:143], a[156:159], a[120:123], v[128:143]\n\t"
    "buffer_load_dwordx4 v227, s[28:31], s80 offen offset:1024 nt lds\n\t"
    "v_exp_f32 v191, v191\n\t"
    "v_cvt_pk_bf16_f32 v39, v174, v175\n\t"
    "v_add_f32 v210, v210, v174\n\t"
    "v_add_f32 v212, v212, v175\n\t"
    "v_mfma_f32_32x32x16_bf16 v[144:159], a[156:159], a[124:127], v[144:159]\n\t"
    "ds_write_b128 v224, v[36:39] offset:37888\n\t"
    "v_cvt_pk_bf16_f32 v199, v190, v191\n\t"
    "v_add_f32 v211, v211, v190\n\t"
    "v_add_f32 v213, v213, v191\n\t"
    "ds_write_b128 v224, v[196:199] offset:39936\n\t"
    "s_waitcnt lgkmcnt(2)\n\t"
    "s_waitcnt lgkmcnt(0)\n\t"
    "s_barrier\n\t"
    "v_mfma_f32_32x32x16_bf16 v[160:175], a[128:131], a[0:3], 0\n\t"
    "buffer_load_dwordx4 v228, s[28:31], s80 offen offset:2048 nt lds\n\t"
    "ds_read_b128 v[40:43], v251 offset:38912\n\t"
    "ds_read_b128 v[48:51], v252 offset:36864\n\t"
    "v_mfma_f32_32x32x16_bf16 v[176:191], a[128:131], a[4:7], 0\n\t"
    "buffer_load_dwordx4 v229, s[28:31], s80 offen offset:3072 nt lds\n\t"
    "ds_read_b128 v[56:59], v253 offset:38912\n\t"
    "ds_read_b128 v[44:47], v251 offset:39936\n\t"
    "v_mfma_f32_32x32x16_bf16 v[160:175], a[132:135], a[8:11], v[160:175]\n\t"
    "s_add_u32 m0, s56, 16384\n\t"
    "s_xor_b32 s80, s68, 16640\n\t"
    "buffer_load_dwordx4 v226, s[28:31], s80 offen nt lds\n\t"
    "v_max3_f32 v208, v208, v128, v129\n\t"
    "v_max3_f32 v209, v209, v144, v145\n\t"
    "v_fma_f32 v128, v128, s69, v214\n\t"
    "v_fma_f32 v129, v129, s69, v214\n\t"
    "v_mfma_f32_32x32x16_bf16 v[176:191], a[132:135], a[12:15], v[176:191]\n\t"
    "v_fma_f32 v144, v144, s69, v215\n\t"
    "v_fma_f32 v145, v145, s69, v215\n\t"
    "v_exp_f32 v128, v128\n\t"
    "v_exp_f32 v129, v129\n\t"
    "s_waitcnt vmcnt(17)\n\t"
    "ds_read_b128 a[144:147], v230 offset:4096\n\t"
    "ds_read_b128 a[148:151], v231 offset:4096\n\t"
    "v_mfma_f32_32x32x16_bf16 v[160:175], a[136:139], a[16:19], v[160:175]\n\t"
    "buffer_load_dwordx4 v227, s[28:31], s80 offen offset:1024 nt lds\n\t"
    "v_exp_f32 v144, v144\n\t"
    "v_exp_f32 v145, v145\n\t"
    "v_cvt_pk_bf16_f32 v64, v128, v129\n\t"
    "v_add_f32 v210, v210, v128\n\t"
    "v_add_f32 v212, v212, v129\n\t"
    "ds_read_b128 a[152:155], v232 offset:4096\n\t"
    "ds_read_b128 a[156:159], v233 offset:4096\n\t"
    "v_mfma_f32_32x32x16_bf16 v[176:191], a[136:139], a[20:23], v[176:191]\n\t"
    "buffer_load_dwordx4 v228, s[28:31], s80 offen offset:2048 nt lds\n\t"
    "ds_read_b128 v[52:55], v252 offset:37888\n\t"
    "ds_read_b128 v[60:63], v253 offset:39936\n\t"
    "v_cvt_pk_bf16_f32 v192, v144, v145\n\t"
    "v_add_f32 v211, v211, v144\n\t"
    "v_add_f32 v213, v213, v145\n\t"
    "v_max3_f32 v208, v208, v130, v131\n\t"
    "v_mfma_f32_32x32x16_bf16 v[160:175], a[140:143], a[24:27], v[160:175]\n\t"
    "v_max3_f32 v209, v209, v146, v147\n\t"
    "v_fma_f32 v130, v130, s69, v214\n\t"
    "v_fma_f32 v131, v131, s69, v214\n\t"
    "v_fma_f32 v146, v146, s69, v215\n\t"
    "v_fma_f32 v147, v147, s69, v215\n\t"
    "v_mfma_f32_32x32x16_bf16 v[176:191], a[140:143], a[28:31], v[176:191]\n\t"
    "v_exp_f32 v130, v130\n\t"
    "v_exp_f32 v131, v131\n\t"
    "v_exp_f32 v146, v146\n\t"
    "v_exp_f32 v147, v147\n\t"
    "s_waitcnt lgkmcnt(2)\n\t"
    "v_mfma_f32_32x32x16_bf16 v[160:175], a[144:147], a[32:35], v[160:175]\n\t"
    "v_cvt_pk_bf16_f32 v65, v130, v131\n\t"
    "v_add_f32 v210, v210, v130\n\t"
    "v_add_f32 v212, v212, v131\n\t"
    "v_cvt_pk_bf16_f32 v193, v146, v147\n\t"
    "v_mfma_f32_32x32x16_bf16 v[176:191], a[144:147], a[36:39], v[176:191]\n\t"
    "v_add_f32 v211, v211, v146\n\t"
    "v_add_f32 v213, v213, v147\n\t"
    "v_max3_f32 v208, v208, v132, v133\n\t"
    "v_max3_f32 v209, v209, v148, v149\n\t"
    "v_fma_f32 v132, v132, s69, v214\n\t"
    "v_mfma_f32_32x32x16_bf16 v[160:175], a[148:151], a[40:43], v[160:175]\n\t"
    "v_fma_f32 v133, v133, s69, v214\n\t"
    "v_fma_f32 v148, v148, s69, v215\n\t"
    "v_fma_f32 v149, v149, s69, v215\n\t"
    "v_exp_f32 v132, v132\n\t"
    "v_mfma_f32_32x32x16_bf16 v[176:191], a[148:151], a[44:47], v[176:191]\n\t"
    "v_exp_f32 v133, v133\n\t"
    "v_exp_f32 v148, v148\n\t"
    "v_exp_f32 v149, v149\n\t"
    "v_cvt_pk_bf16_f32 v66, v132, v133\n\t"
    "v_add_f32 v210, v210, v132\n\t"
    "s_waitcnt vmcnt(15)\n\t"
    "ds_read_b128 a[128:131], v230 offset:24576\n\t"
    "ds_read_b128 a[132:135], v231 offset:24576\n\t"
    "v_mfma_f32_32x32x16_bf16 v[160:175], a[152:155], a[48:51], v[160:175]\n\t"
    "buffer_load_dwordx4 v229, s[28:31], s80 offen offset:3072 nt lds\n\t"
    "v_add_f32 v212, v212, v133\n\t"
    "v_cvt_pk_bf16_f32 v194, v148, v149\n\t"
    "v_add_f32 v211, v211, v148\n\t"
    "v_add_f32 v213, v213, v149\n\t"
    "ds_read_b128 a[136:139], v232 offset:24576\n\t"
    "ds_read_b128 a[140:143], v233 offset:24576\n\t"
    "v_mfma_f32_32x32x16_bf16 v[176:191], a[152:155], a[52:55], v[176:191]\n\t"
    "v_max3_f32 v208, v208, v134, v135\n\t"
    "v_max3_f32 v209, v209, v150, v151\n\t"
    "v_fma_f32 v134, v134, s69, v214\n\t"
    "v_fma_f32 v135, v135, s69, v214\n\t"
    "v_mfma_f32_32x32x16_bf16 v[160:175], a[156:159], a[56:59], v[160:175]\n\t"
    "s_add_u32 m0, s56, 20480\n\t"
    "s_xor_b32 s80, s68, 16768\n\t"
    "buffer_load_dwordx4 v226, s[28:31], s80 offen nt lds\n\t"
    "v_fma_f32 v150, v150, s69, v215\n\t"
    "v_fma_f32 v151, v151, s69, v215\n\t"
    "v_exp_f32 v134, v134\n\t"
    "v_exp_f32 v135, v135\n\t"
    "v_exp_f32 v150, v150\n\t"
    "v_mfma_f32_32x32x16_bf16 v[176:191], a[156:159], a[60:63], v[176:191]\n\t"
    "v_exp_f32 v151, v151\n\t"
    "v_cvt_pk_bf16_f32 v67, v134, v135\n\t"
    "v_add_f32 v210, v210, v134\n\t"
    "v_add_f32 v212, v212, v135\n\t"
    "s_waitcnt lgkmcnt(0)\n\t"
    "v_mfma_f32_32x32x16_bf16 v[160:175], a[128:131], a[64:67], v[160:175]\n\t"
    "buffer_load_dwordx4 v227, s[28:31], s80 offen offset:1024 nt lds\n\t"
    "ds_write_b128 v224, v[64:67] offset:32768\n\t"
    "v_cvt_pk_bf16_f32 v195, v150, v151\n\t"
    "v_add_f32 v211, v211, v150\n\t"
    "v_add_f32 v213, v213, v151\n\t"
    "ds_write_b128 v224, v[192:195] offset:34816\n\t"
    "v_mfma_f32_32x32x16_bf16 v[176:191], a[128:131], a[68:71], v[176:191]\n\t"
    "v_max3_f32 v208, v208, v136, v137\n\t"
    "v_max3_f32 v209, v209, v152, v153\n\t"
    "v_fma_f32 v136, v136, s69, v214\n\t"
    "v_fma_f32 v137, v137, s69, v214\n\t"
    "v_mfma_f32_32x32x16_bf16 v[160:175], a[132:135], a[72:75], v[160:175]\n\t"
    "buffer_load_dwordx4 v228, s[28:31], s80 offen offset:2048 nt lds\n\t"
    "v_fma_f32 v152, v152, s69, v215\n\t"
    "v_fma_f32 v153, v153, s69, v215\n\t"
    "v_exp_f32 v136, v136\n\t"
    "v_exp_f32 v137, v137\n\t"
    "v_mfma_f32_32x32x16_bf16 v[176:191], a[132:135], a[76:79], v[176:191]\n\t"
    "v_exp_f32 v152, v152\n\t"
    "v_exp_f32 v153, v153\n\t"
    "v_cvt_pk_bf16_f32 v68, v136, v137\n\t"
    "v_add_f32 v210, v210, v136\n\t"
    "v_add_f32 v212, v212, v137\n\t"
    "s_waitcnt vmcnt(15)\n\t"
    "ds_read_b128 a[144:147], v230 offset:28672\n\t"
    "ds_read_b128 a[148:151], v231 offset:28672\n\t"
    "v_mfma_f32_32x32x16_bf16 v[160:175], a[136:139], a[80:83], v[160:175]\n\t"
    "buffer_load_dwordx4 v229, s[28:31], s80 offen offset:3072 nt lds\n\t"
    "v_cvt_pk_bf16_f32 v196, v152, v153\n\t"
    "v_add_f32 v211, v211, v152\n\t"
    "v_add_f32 v213, v213, v153\n\t"
    "v_max3_f32 v208, v208, v138, v139\n\t"
    "ds_read_b128 a[152:155], v232 offset:28672\n\t"
    "ds_read_b128 a[156:159], v233 offset:28672\n\t"
    "v_mfma_f32_32x32x16_bf16 v[176:191], a[136:139], a[84:87], v[176:191]\n\t"
    "v_max3_f32 v209, v209, v154, v155\n\t"
    "v_fma_f32 v138, v138, s69, v214\n\t"
    "v_fma_f32 v139, v139, s69, v214\n\t"
    "v_fma_f32 v154, v154, s69, v215\n\t"
    "v_fma_f32 v155, v155, s69, v215\n\t"
    "v_mfma_f32_32x32x16_bf16 v[160:175], a[140:143], a[88:91], v[160:175]\n\t"
    "v_exp_f32 v138, v138\n\t"
    "v_exp_f32 v139, v139\n\t"
    "v_exp_f32 v154, v154\n\t"
    "v_exp_f32 v155, v155\n\t"
    "v_mfma_f32_32x32x16_bf16 v[176:191], a[140:143], a[92:95], v[176:191]\n\t"
    "s_add_u32 m0, s56, 0\n\t"
    "s_xor_b32 s80, s68, 24576\n\t"
    "buffer_load_dwordx4 v226, s[28:31], s80 offen nt lds\n\t"
    "v_cvt_pk_bf16_f32 v69, v138, v139\n\t"
    "v_add_f32 v210, v210, v138\n\t"
    "v_add_f32 v212, v212, v139\n\t"
    "v_cvt_pk_bf16_f32 v197, v154, v155\n\t"
    "s_waitcnt lgkmcnt(0)\n\t"
    "v_mfma_f32_32x32x16_bf16 v[160:175], a[144:147], a[96:99], v[160:175]\n\t"
    "v_add_f32 v211, v211, v154\n\t"
    "v_add_f32 v213, v213, v155\n\t"
    "v_max3_f32 v208, v208, v140, v141\n\t"
    "v_max3_f32 v209, v209, v156, v157\n\t"
    "v_fma_f32 v140, v140, s69, v214\n\t"
    "v_mfma_f32_32x32x16_bf16 v[176:191], a[144:147], a[100:103], v[176:191]\n\t"
    "buffer_load_dwordx4 v227, s[28:31], s80 offen offset:1024 nt lds\n\t"
    "v_fma_f32 v141, v141, s69, v214\n\t"
    "v_fma_f32 v156, v156, s69, v215\n\t"
    "v_fma_f32 v157, v157, s69, v215\n\t"
    "v_exp_f32 v140, v140\n\t"
    "v_mfma_f32_32x32x16_bf16 v[160:175], a[148:151], a[104:107], v[160:175]\n\t"
    "v_exp_f32 v141, v141\n\t"
    "v_exp_f32 v156, v156\n\t"
    "v_exp_f32 v157, v157\n\t"
    "v_cvt_pk_bf16_f32 v70, v140, v141\n\t"
    "v_add_f32 v210, v210, v140\n\t"
    "v_mfma_f32_32x32x16_bf16 v[176:191], a[148:151], a[108:111], v[176:191]\n\t"
    "buffer_load_dwordx4 v228, s[28:31], s80 offen offset:2048 nt lds\n\t"
    "v_add_f32 v212, v212, v141\n\t"
    "v_cvt_pk_bf16_f32 v198, v156, v157\n\t"
    "v_add_f32 v211, v211, v156\n\t"
    "v_add_f32 v213, v213, v157\n\t"
    "s_waitcnt vmcnt(15)\n\t"
    "ds_read_b128 a[128:131], v230 offset:8192\n\t"
    "ds_read_b128 a[132:135], v231 offset:8192\n\t"
    "v_mfma_f32_32x32x16_bf16 v[160:175], a[152:155], a[112:115], v[160:175]\n\t"
    "v_max3_f32 v208, v208, v142, v143\n\t"
    "v_max3_f32 v209, v209, v158, v159\n\t"
    "v_fma_f32 v142, v142, s69, v214\n\t"
    "v_fma_f32 v143, v143, s69, v214\n\t"
    "ds_read_b128 a[136:139], v232 offset:8192\n\t"
    "ds_read_b128 a[140:143], v233 offset:8192\n\t"
    "v_mfma_f32_32x32x16_bf16 v[176:191], a[152:155], a[116:119], v[176:191]\n\t"
    "v_fma_f32 v158, v158, s69, v215\n\t"
    "v_fma_f32 v159, v159, s69, v215\n\t"
    "v_exp_f32 v142, v142\n\t"
    "v_exp_f32 v143, v143\n\t"
    "v_exp_f32 v158, v158\n\t"
    "v_mfma_f32_32x32x16_bf16 v[160:175], a[156:159], a[120:123], v[160:175]\n\t"
    "v_exp_f32 v159, v159\n\t"
    "v_cvt_pk_bf16_f32 v71, v142, v143\n\t"
    "v_add_f32 v210, v210, v142\n\t"
    "v_add_f32 v212, v212, v143\n\t"
    "v_mfma_f32_32x32x16_bf16 v[176:191], a[156:159], a[124:127], v[176:191]\n\t"
    "ds_write_b128 v224, v[68:71] offset:33792\n\t"
    "v_cvt_pk_bf16_f32 v199, v158, v159\n\t"
    "v_add_f32 v211, v211, v158\n\t"
    "v_add_f32 v213, v213, v159\n\t"
    "ds_write_b128 v224, v[196:199] offset:35840\n\t"
    "s_waitcnt lgkmcnt(2)\n\t"
    "s_waitcnt lgkmcnt(0)\n\t"
    "s_barrier\n\t"
    "v_mfma_f32_32x32x16_bf16 v[128:143], a[128:131], a[0:3], 0\n\t"
    "buffer_load_dwordx4 v229, s[28:31], s80 offen offset:3072 nt lds\n\t"
    "ds_read_b128 v[72:75], v251 offset:34816\n\t"
    "ds_read_b128 v[80:83], v252 offset:32768\n\t"
    "v_mfma_f32_32x32x16_bf16 v[144:159], a[128:131], a[4:7], 0\n\t"
    "s_add_u32 m0, s56, 4096\n\t"
    "s_xor_b32 s80, s68, 24704\n\t"
    "buffer_load_dwordx4 v226, s[28:31], s80 offen nt lds\n\t"
    "ds_read_b128 v[88:91], v253 offset:34816\n\t"
    "ds_read_b128 v[76:79], v251 offset:35840\n\t"
    "v_mfma_f32_32x32x16_bf16 v[128:143], a[132:135], a[8:11], v[128:143]\n\t"
    "buffer_load_dwordx4 v227, s[28:31], s80 offen offset:1024 nt lds\n\t"
    "v_max3_f32 v208, v208, v160, v161\n\t"
    "v_max3_f32 v209, v209, v176, v177\n\t"
    "v_fma_f32 v160, v160, s69, v214\n\t"
    "v_fma_f32 v161, v161, s69, v214\n\t"
    "v_mfma_f32_32x32x16_bf16 v[144:159], a[132:135], a[12:15], v[144:159]\n\t"
    "v_fma_f32 v176, v176, s69, v215\n\t"
    "v_fma_f32 v177, v177, s69, v215\n\t"
    "v_exp_f32 v160, v160\n\t"
    "v_exp_f32 v161, v161\n\t"
    "s_waitcnt vmcnt(14)\n\t"
    "ds_read_b128 a[144:147], v230 offset:12288\n\t"
    "ds_read_b128 a[148:151], v231 offset:12288\n\t"
    "v_mfma_f32_32x32x16_bf16 v[128:143], a[136:139], a[16:19], v[128:143]\n\t"
    "buffer_load_dwordx4 v228, s[28:31], s80 offen offset:2048 nt lds\n\t"
    "v_exp_f32 v176, v176\n\t"
    "v_exp_f32 v177, v177\n\t"
    "v_cvt_pk_bf16_f32 v96, v160, v161\n\t"
    "v_add_f32 v210, v210, v160\n\t"
    "v_add_f32 v212, v212, v161\n\t"
    "ds_read_b128 a[152:155], v232 offset:12288\n\t"
    "ds_read_b128 a[156:159], v233 offset:12288\n\t"
    "v_mfma_f32_32x32x16_bf16 v[144:159], a[136:139], a[20:23], v[144:159]\n\t"
    "ds_read_b128 v[84:87], v252 offset:33792\n\t"
    "ds_read_b128 v[92:95], v253 offset:35840\n\t"
    "v_cvt_pk_bf16_f32 v192, v176, v177\n\t"
    "v_add_f32 v211, v211, v176\n\t"
    "v_add_f32 v213, v213, v177\n\t"
    "v_max3_f32 v208, v208, v162, v163\n\t"
    "v_mfma_f32_32x32x16_bf16 v[128:143], a[140:143], a[24:27], v[128:143]\n\t"
    "buffer_load_dwordx4 v229, s[28:31], s80 offen offset:3072 nt lds\n\t"
    "v_max3_f32 v209, v209, v178, v179\n\t"
    "v_fma_f32 v162, v162, s69, v214\n\t"
    "v_fma_f32 v163, v163, s69, v214\n\t"
    "v_fma_f32 v178, v178, s69, v215\n\t"
    "v_fma_f32 v179, v179, s69, v215\n\t"
    "v_mfma_f32_32x32x16_bf16 v[144:159], a[140:143], a[28:31], v[144:159]\n\t"
    "v_exp_f32 v162, v162\n\t"
    "v_exp_f32 v163, v163\n\t"
    "v_exp_f32 v178, v178\n\t"
    "v_exp_f32 v179, v179\n\t"
    "s_waitcnt lgkmcnt(2)\n\t"
    "v_mfma_f32_32x32x16_bf16 v[128:143], a[144:147], a[32:35], v[128:143]\n\t"
    "v_cvt_pk_bf16_f32 v97, v162, v163\n\t"
    "v_add_f32 v210, v210, v162\n\t"
    "v_add_f32 v212, v212, v163\n\t"
    "v_cvt_pk_bf16_f32 v193, v178, v179\n\t"
    "v_mfma_f32_32x32x16_bf16 v[144:159], a[144:147], a[36:39], v[144:159]\n\t"
    "s_add_u32 m0, s56, 24576\n\t"
    "s_xor_b32 s80, s68, 24832\n\t"
    "buffer_load_dwordx4 v226, s[28:31], s80 offen nt lds\n\t"
    "v_add_f32 v211, v211, v178\n\t"
    "v_add_f32 v213, v213, v179\n\t"
    "v_max3_f32 v208, v208, v164, v165\n\t"
    "v_max3_f32 v209, v209, v180, v181\n\t"
    "v_fma_f32 v164, v164, s69, v214\n\t"
    "v_mfma_f32_32x32x16_bf16 v[128:143], a[148:151], a[40:43], v[128:143]\n\t"
    "v_fma_f32 v165, v165, s69, v214\n\t"
    "v_fma_f32 v180, v180, s69, v215\n\t"
    "v_fma_f32 v181, v181, s69, v215\n\t"
    "v_exp_f32 v164, v164\n\t"
    "v_mfma_f32_32x32x16_bf16 v[144:159], a[148:151], a[44:47], v[144:159]\n\t"
    "buffer_load_dwordx4 v227, s[28:31], s80 offen offset:1024 nt lds\n\t"
    "v_exp_f32 v165, v165\n\t"
    "v_exp_f32 v180, v180\n\t"
    "v_exp_f32 v181, v181\n\t"
    "v_cvt_pk_bf16_f32 v98, v164, v165\n\t"
    "v_add_f32 v210, v210, v164\n\t"
    "s_waitcnt vmcnt(14)\n\t"
    "ds_read_b128 a[128:131], v230 offset:16384\n\t"
    "ds_read_b128 a[132:135], v231 offset:16384\n\t"
    "v_mfma_f32_32x32x16_bf16 v[128:143], a[152:155], a[48:51], v[128:143]\n\t"
    "v_add_f32 v212, v212, v165\n\t"
    "v_cvt_pk_bf16_f32 v194, v180, v181\n\t"
    "v_add_f32 v211, v211, v180\n\t"
    "v_add_f32 v213, v213, v181\n\t"
    "ds_read_b128 a[136:139], v232 offset:16384\n\t"
    "ds_read_b128 a[140:143], v233 offset:16384\n\t"
    "v_mfma_f32_32x32x16_bf16 v[144:159], a[152:155], a[52:55], v[144:159]\n\t"
    "buffer_load_dwordx4 v228, s[28:31], s80 offen offset:2048 nt lds\n\t"
    "v_max3_f32 v208, v208, v166, v167\n\t"
    "v_max3_f32 v209, v209, v182, v183\n\t"
    "v_fma_f32 v166, v166, s69, v214\n\t"
    "v_fma_f32 v167, v167, s69, v214\n\t"
    "v_mfma_f32_32x32x16_bf16 v[128:143], a[156:159], a[56:59], v[128:143]\n\t"
    "v_fma_f32 v182, v182, s69, v215\n\t"
    "v_fma_f32 v183, v183, s69, v215\n\t"
    "v_exp_f32 v166, v166\n\t"
    "v_exp_f32 v167, v167\n\t"
    "v_exp_f32 v182, v182\n\t"
    "v_mfma_f32_32x32x16_bf16 v[144:159], a[156:159], a[60:63], v[144:159]\n\t"
    "v_exp_f32 v183, v183\n\t"
    "v_cvt_pk_bf16_f32 v99, v166, v167\n\t"
    "v_add_f32 v210, v210, v166\n\t"
    "v_add_f32 v212, v212, v167\n\t"
    "s_waitcnt lgkmcnt(0)\n\t"
    "v_mfma_f32_32x32x16_bf16 v[128:143], a[128:131], a[64:67], v[128:143]\n\t"
    "ds_write_b128 v224, v[96:99] offset:36864\n\t"
    "v_cvt_pk_bf16_f32 v195, v182, v183\n\t"
    "v_add_f32 v211, v211, v182\n\t"
    "v_add_f32 v213, v213, v183\n\t"
    "ds_write_b128 v224, v[192:195] offset:38912\n\t"
    "v_mfma_f32_32x32x16_bf16 v[144:159], a[128:131], a[68:71], v[144:159]\n\t"
    "v_max3_f32 v208, v208, v168, v169\n\t"
    "v_max3_f32 v209, v209, v184, v185\n\t"
    "v_fma_f32 v168, v168, s69, v214\n\t"
    "v_fma_f32 v169, v169, s69, v214\n\t"
    "v_mfma_f32_32x32x16_bf16 v[128:143], a[132:135], a[72:75], v[128:143]\n\t"
    "v_fma_f32 v184, v184, s69, v215\n\t"
    "v_fma_f32 v185, v185, s69, v215\n\t"
    "v_exp_f32 v168, v168\n\t"
    "v_exp_f32 v169, v169\n\t"
    "v_mfma_f32_32x32x16_bf16 v[144:159], a[132:135], a[76:79], v[144:159]\n\t"
    "buffer_load_dwordx4 v229, s[28:31], s80 offen offset:3072 nt lds\n\t"
    "v_exp_f32 v184, v184\n\t"
    "v_exp_f32 v185, v185\n\t"
    "v_cvt_pk_bf16_f32 v100, v168, v169\n\t"
    "v_add_f32 v210, v210, v168\n\t"
    "v_add_f32 v212, v212, v169\n\t"
    "s_waitcnt vmcnt(12)\n\t"
    "ds_read_b128 a[144:147], v230 offset:20480\n\t"
    "ds_read_b128 a[148:151], v231 offset:20480\n\t"
    "v_mfma_f32_32x32x16_bf16 v[128:143], a[136:139], a[80:83], v[128:143]\n\t"
    "v_cvt_pk_bf16_f32 v196, v184, v185\n\t"
    "v_add_f32 v211, v211, v184\n\t"
    "v_add_f32 v213, v213, v185\n\t"
    "v_max3_f32 v208, v208, v170, v171\n\t"
    "ds_read_b128 a[152:155], v232 offset:20480\n\t"
    "ds_read_b128 a[156:159], v233 offset:20480\n\t"
    "v_mfma_f32_32x32x16_bf16 v[144:159], a[136:139], a[84:87], v[144:159]\n\t"
    "s_add_u32 m0, s56, 28672\n\t"
    "s_xor_b32 s80, s68, 24960\n\t"
    "buffer_load_dwordx4 v226, s[28:31], s80 offen nt lds\n\t"
    "v_max3_f32 v209, v209, v186, v187\n\t"
    "v_fma_f32 v170, v170, s69, v214\n\t"
    "v_fma_f32 v171, v171, s69, v214\n\t"
    "v_fma_f32 v186, v186, s69, v215\n\t"
    "v_fma_f32 v187, v187, s69, v215\n\t"
    "v_mfma_f32_32x32x16_bf16 v[128:143], a[140:143], a[88:91], v[128:143]\n\t"
    "v_exp_f32 v170, v170\n\t"
    "v_exp_f32 v171, v171\n\t"
    "v_exp_f32 v186, v186\n\t"
    "v_exp_f32 v187, v187\n\t"
    "v_mfma_f32_32x32x16_bf16 v[144:159], a[140:143], a[92:95], v[144:159]\n\t"
    "buffer_load_dwordx4 v227, s[28:31], s80 offen offset:1024 nt lds\n\t"
    "v_cvt_pk_bf16_f32 v101, v170, v171\n\t"
    "v_add_f32 v210, v210, v170\n\t"
    "v_add_f32 v212, v212, v171\n\t"
    "v_cvt_pk_bf16_f32 v197, v186, v187\n\t"
    "s_waitcnt lgkmcnt(0)\n\t"
    "v_mfma_f32_32x32x16_bf16 v[128:143], a[144:147], a[96:99], v[128:143]\n\t"
    "v_add_f32 v211, v211, v186\n\t"
    "v_add_f32 v213, v213, v187\n\t"
    "v_max3_f32 v208, v208, v172, v173\n\t"
    "v_max3_f32 v209, v209, v188, v189\n\t"
    "v_fma_f32 v172, v172, s69, v214\n\t"
    "v_mfma_f32_32x32x16_bf16 v[144:159], a[144:147], a[100:103], v[144:159]\n\t"
    "v_fma_f32 v173, v173, s69, v214\n\t"
    "v_fma_f32 v188, v188, s69, v215\n\t"
    "v_fma_f32 v189, v189, s69, v215\n\t"
    "v_exp_f32 v172, v172\n\t"
    "v_mfma_f32_32x32x16_bf16 v[128:143], a[148:151], a[104:107], v[128:143]\n\t"
    "buffer_load_dwordx4 v228, s[28:31], s80 offen offset:2048 nt lds\n\t"
    "v_exp_f32 v173, v173\n\t"
    "v_exp_f32 v188, v188\n\t"
    "v_exp_f32 v189, v189\n\t"
    "v_cvt_pk_bf16_f32 v102, v172, v173\n\t"
    "v_add_f32 v210, v210, v172\n\t"
    "v_mfma_f32_32x32x16_bf16 v[144:159], a[148:151], a[108:111], v[144:159]\n\t"
    "v_add_f32 v212, v212, v173\n\t"
    "v_cvt_pk_bf16_f32 v198, v188, v189\n\t"
    "v_add_f32 v211, v211, v188\n\t"
    "v_add_f32 v213, v213, v189\n\t"
    "s_waitcnt vmcnt(11)\n\t"
    "ds_read_b128 a[128:131], v230 offset:0\n\t"
    "ds_read_b128 a[132:135], v231 offset:0\n\t"
    "v_mfma_f32_32x32x16_bf16 v[128:143], a[152:155], a[112:115], v[128:143]\n\t"
    "buffer_load_dwordx4 v229, s[28:31], s80 offen offset:3072 nt lds\n\t"
    "v_max3_f32 v208, v208, v174, v175\n\t"
    "v_max3_f32 v209, v209, v190, v191\n\t"
    "v_fma_f32 v174, v174, s69, v214\n\t"
    "v_fma_f32 v175, v175, s69, v214\n\t"
    "ds_read_b128 a[136:139], v232 offset:0\n\t"
    "ds_read_b128 a[140:143], v233 offset:0\n\t"
    "v_mfma_f32_32x32x16_bf16 v[144:159], a[152:155], a[116:119], v[144:159]\n\t"
    "v_fma_f32 v190, v190, s69, v215\n\t"
    "v_fma_f32 v191, v191, s69, v215\n\t"
    "v_exp_f32 v174, v174\n\t"
    "v_exp_f32 v175, v175\n\t"
    "v_exp_f32 v190, v190\n\t"
    "v_mfma_f32_32x32x16_bf16 v[128:143], a[156:159], a[120:123], v[128:143]\n\t"
    "s_add_u32 m0, s56, 8192\n\t"
    "s_xor_b32 s80, s68, 147456\n\t"
    "buffer_load_dwordx4 v226, s[28:31], s80 offen nt lds\n\t"
    "v_exp_f32 v191, v191\n\t"
    "v_cvt_pk_bf16_f32 v103, v174, v175\n\t"
    "v_add_f32 v210, v210, v174\n\t"
    "v_add_f32 v212, v212, v175\n\t"
    "v_mfma_f32_32x32x16_bf16 v[144:159], a[156:159], a[124:127], v[144:159]\n\t"
    "ds_write_b128 v224, v[100:103] offset:37888\n\t"
    "v_cvt_pk_bf16_f32 v199, v190, v191\n\t"
    "v_add_f32 v211, v211, v190\n\t"
    "v_add_f32 v213, v213, v191\n\t"
    "ds_write_b128 v224, v[196:199] offset:39936\n\t"
    "s_waitcnt lgkmcnt(2)\n\t"
    "s_waitcnt lgkmcnt(0)\n\t"
    "s_barrier\n\t"
    "v_mfma_f32_32x32x16_bf16 v[160:175], a[128:131], a[0:3], 0\n\t"
    "buffer_load_dwordx4 v227, s[28:31], s80 offen offset:1024 nt lds\n\t"
    "ds_read_b128 a[160:163], v251 offset:38912\n\t"
    "ds_read_b128 a[168:171], v252 offset:36864\n\t"
    "v_mfma_f32_32x32x16_bf16 v[176:191], a[128:131], a[4:7], 0\n\t"
    "buffer_load_dwordx4 v228, s[28:31], s80 offen offset:2048 nt lds\n\t"
    "ds_read_b128 a[176:179], v253 offset:38912\n\t"
    "ds_read_b128 a[164:167], v251 offset:39936\n\t"
    "v_mfma_f32_32x32x16_bf16 v[160:175], a[132:135], a[8:11], v[160:175]\n\t"
    "v_max3_f32 v208, v208, v128, v129\n\t"
    "v_max3_f32 v209, v209, v144, v145\n\t"
    "v_fma_f32 v128, v128, s69, v214\n\t"
    "v_fma_f32 v129, v129, s69, v214\n\t"
    "v_mfma_f32_32x32x16_bf16 v[176:191], a[132:135], a[12:15], v[176:191]\n\t"
    "v_fma_f32 v144, v144, s69, v215\n\t"
    "v_fma_f32 v145, v145, s69, v215\n\t"
    "v_exp_f32 v128, v128\n\t"
    "v_exp_f32 v129, v129\n\t"
    "buffer_load_dwordx4 v229, s[28:31], s80 offen offset:3072 nt lds\n\t"
    "s_waitcnt vmcnt(12)\n\t"
    "ds_read_b128 a[144:147], v230 offset:4096\n\t"
    "ds_read_b128 a[148:151], v231 offset:4096\n\t"
    "v_mfma_f32_32x32x16_bf16 v[160:175], a[136:139], a[16:19], v[160:175]\n\t"
    "v_exp_f32 v144, v144\n\t"
    "v_exp_f32 v145, v145\n\t"
    "v_cvt_pk_bf16_f32 v104, v128, v129\n\t"
    "v_add_f32 v210, v210, v128\n\t"
    "v_add_f32 v212, v212, v129\n\t"
    "ds_read_b128 a[152:155], v232 offset:4096\n\t"
    "ds_read_b128 a[156:159], v233 offset:4096\n\t"
    "v_mfma_f32_32x32x16_bf16 v[176:191], a[136:139], a[20:23], v[176:191]\n\t"
    "ds_read_b128 a[172:175], v252 offset:37888\n\t"
    "ds_read_b128 a[180:183], v253 offset:39936\n\t"
    "v_cvt_pk_bf16_f32 v192, v144, v145\n\t"
    "v_add_f32 v211, v211, v144\n\t"
    "v_add_f32 v213, v213, v145\n\t"
    "v_max3_f32 v208, v208, v130, v131\n\t"
    "v_mfma_f32_32x32x16_bf16 v[160:175], a[140:143], a[24:27], v[160:175]\n\t"
    "s_add_u32 m0, s56, 12288\n\t"
    "s_xor_b32 s80, s68, 147584\n\t"
    "buffer_load_dwordx4 v226, s[28:31], s80 offen nt lds\n\t"
    "v_max3_f32 v209, v209, v146, v147\n\t"
    "v_fma_f32 v130, v130, s69, v214\n\t"
    "v_fma_f32 v131, v131, s69, v214\n\t"
    "v_fma_f32 v146, v146, s69, v215\n\t"
    "v_fma_f32 v147, v147, s69, v215\n\t"
    "v_mfma_f32_32x32x16_bf16 v[176:191], a[140:143], a[28:31], v[176:191]\n\t"
    "v_exp_f32 v130, v130\n\t"
    "v_exp_f32 v131, v131\n\t"
    "v_exp_f32 v146, v146\n\t"
    "v_exp_f32 v147, v147\n\t"
    "s_waitcnt lgkmcnt(2)\n\t"
    "v_mfma_f32_32x32x16_bf16 v[160:175], a[144:147], a[32:35], v[160:175]\n\t"
    "buffer_load_dwordx4 v227, s[28:31], s80 offen offset:1024 nt lds\n\t"
    "v_cvt_pk_bf16_f32 v105, v130, v131\n\t"
    "v_add_f32 v210, v210, v130\n\t"
    "v_add_f32 v212, v212, v131\n\t"
    "v_cvt_pk_bf16_f32 v193, v146, v147\n\t"
    "v_mfma_f32_32x32x16_bf16 v[176:191], a[144:147], a[36:39], v[176:191]\n\t"
    "v_add_f32 v211, v211, v146\n\t"
    "v_add_f32 v213, v213, v147\n\t"
    "v_max3_f32 v208, v208, v132, v133\n\t"
    "v_max3_f32 v209, v209, v148, v149\n\t"
    "v_fma_f32 v132, v132, s69, v214\n\t"
    "v_mfma_f32_32x32x16_bf16 v[160:175], a[148:151], a[40:43], v[160:175]\n\t"
    "v_fma_f32 v133, v133, s69, v214\n\t"
    "v_fma_f32 v148, v148, s69, v215\n\t"
    "v_fma_f32 v149, v149, s69, v215\n\t"
    "v_exp_f32 v132, v132\n\t"
    "v_mfma_f32_32x32x16_bf16 v[176:191], a[148:151], a[44:47], v[176:191]\n\t"
    "buffer_load_dwordx4 v228, s[28:31], s80 offen offset:2048 nt lds\n\t"
    "v_exp_f32 v133, v133\n\t"
    "v_exp_f32 v148, v148\n\t"
    "v_exp_f32 v149, v149\n\t"
    "v_cvt_pk_bf16_f32 v106, v132, v133\n\t"
    "v_add_f32 v210, v210, v132\n\t"
    "buffer_load_dwordx4 v229, s[28:31], s80 offen offset:3072 nt lds\n\t"
    "s_waitcnt vmcnt(12)\n\t"
    "ds_read_b128 a[128:131], v230 offset:24576\n\t"
    "ds_read_b128 a[132:135], v231 offset:24576\n\t"
    "v_mfma_f32_32x32x16_bf16 v[160:175], a[152:155], a[48:51], v[160:175]\n\t"
    "v_add_f32 v212, v212, v133\n\t"
    "v_cvt_pk_bf16_f32 v194, v148, v149\n\t"
    "v_add_f32 v211, v211, v148\n\t"
    "v_add_f32 v213, v213, v149\n\t"
    "ds_read_b128 a[136:139], v232 offset:24576\n\t"
    "ds_read_b128 a[140:143], v233 offset:24576\n\t"
    "v_mfma_f32_32x32x16_bf16 v[176:191], a[152:155], a[52:55], v[176:191]\n\t"
    "v_max3_f32 v208, v208, v134, v135\n\t"
    "v_max3_f32 v209, v209, v150, v151\n\t"
    "v_fma_f32 v134, v134, s69, v214\n\t"
    "v_fma_f32 v135, v135, s69, v214\n\t"
    "v_mfma_f32_32x32x16_bf16 v[160:175], a[156:159], a[56:59], v[160:175]\n\t"
    "s_add_u32 m0, s56, 16384\n\t"
    "s_xor_b32 s80, s68, 147712\n\t"
    "buffer_load_dwordx4 v226, s[28:31], s80 offen nt lds\n\t"
    "v_fma_f32 v150, v150, s69, v215\n\t"
    "v_fma_f32 v151, v151, s69, v215\n\t"
    "v_exp_f32 v134, v134\n\t"
    "v_exp_f32 v135, v135\n\t"
    "v_exp_f32 v150, v150\n\t"
    "v_mfma_f32_32x32x16_bf16 v[176:191], a[156:159], a[60:63], v[176:191]\n\t"
    "v_exp_f32 v151, v151\n\t"
    "v_cvt_pk_bf16_f32 v107, v134, v135\n\t"
    "v_add_f32 v210, v210, v134\n\t"
    "v_add_f32 v212, v212, v135\n\t"
    "s_waitcnt lgkmcnt(0)\n\t"
    "v_mfma_f32_32x32x16_bf16 v[160:175], a[128:131], a[64:67], v[160:175]\n\t"
    "buffer_load_dwordx4 v227, s[28:31], s80 offen offset:1024 nt lds\n\t"
    "ds_write_b128 v224, v[104:107] offset:32768\n\t"
    "v_cvt_pk_bf16_f32 v195, v150, v151\n\t"
    "v_add_f32 v211, v211, v150\n\t"
    "v_add_f32 v213, v213, v151\n\t"
    "ds_write_b128 v224, v[192:195] offset:34816\n\t"
    "v_mfma_f32_32x32x16_bf16 v[176:191], a[128:131], a[68:71], v[176:191]\n\t"
    "v_max3_f32 v208, v208, v136, v137\n\t"
    "v_max3_f32 v209, v209, v152, v153\n\t"
    "v_fma_f32 v136, v136, s69, v214\n\t"
    "v_fma_f32 v137, v137, s69, v214\n\t"
    "v_mfma_f32_32x32x16_bf16 v[160:175], a[132:135], a[72:75], v[160:175]\n\t"
    "v_fma_f32 v152, v152, s69, v215\n\t"
    "v_fma_f32 v153, v153, s69, v215\n\t"
    "v_exp_f32 v136, v136\n\t"
    "v_exp_f32 v137, v137\n\t"
    "v_mfma_f32_32x32x16_bf16 v[176:191], a[132:135], a[76:79], v[176:191]\n\t"
    "buffer_load_dwordx4 v228, s[28:31], s80 offen offset:2048 nt lds\n\t"
    "v_exp_f32 v152, v152\n\t"
    "v_exp_f32 v153, v153\n\t"
    "v_cvt_pk_bf16_f32 v108, v136, v137\n\t"
    "v_add_f32 v210, v210, v136\n\t"
    "v_add_f32 v212, v212, v137\n\t"
    "buffer_load_dwordx4 v229, s[28:31], s80 offen offset:3072 nt lds\n\t"
    "s_waitcnt vmcnt(12)\n\t"
    "ds_read_b128 a[144:147], v230 offset:28672\n\t"
    "ds_read_b128 a[148:151], v231 offset:28672\n\t"
    "v_mfma_f32_32x32x16_bf16 v[160:175], a[136:139], a[80:83], v[160:175]\n\t"
    "v_cvt_pk_bf16_f32 v196, v152, v153\n\t"
    "v_add_f32 v211, v211, v152\n\t"
    "v_add_f32 v213, v213, v153\n\t"
    "v_max3_f32 v208, v208, v138, v139\n\t"
    "ds_read_b128 a[152:155], v232 offset:28672\n\t"
    "ds_read_b128 a[156:159], v233 offset:28672\n\t"
    "v_mfma_f32_32x32x16_bf16 v[176:191], a[136:139], a[84:87], v[176:191]\n\t"
    "v_max3_f32 v209, v209, v154, v155\n\t"
    "v_fma_f32 v138, v138, s69, v214\n\t"
    "v_fma_f32 v139, v139, s69, v214\n\t"
    "v_fma_f32 v154, v154, s69, v215\n\t"
    "v_fma_f32 v155, v155, s69, v215\n\t"
    "v_mfma_f32_32x32x16_bf16 v[160:175], a[140:143], a[88:91], v[160:175]\n\t"
    "s_add_u32 m0, s56, 20480\n\t"
    "s_xor_b32 s80, s68, 147840\n\t"
    "buffer_load_dwordx4 v226, s[28:31], s80 offen nt lds\n\t"
    "v_exp_f32 v138, v138\n\t"
    "v_exp_f32 v139, v139\n\t"
    "v_exp_f32 v154, v154\n\t"
    "v_exp_f32 v155, v155\n\t"
    "v_mfma_f32_32x32x16_bf16 v[176:191], a[140:143], a[92:95], v[176:191]\n\t"
    "v_cvt_pk_bf16_f32 v109, v138, v139\n\t"
    "v_add_f32 v210, v210, v138\n\t"
    "v_add_f32 v212, v212, v139\n\t"
    "v_cvt_pk_bf16_f32 v197, v154, v155\n\t"
    "s_waitcnt lgkmcnt(0)\n\t"
    "v_mfma_f32_32x32x16_bf16 v[160:175], a[144:147], a[96:99], v[160:175]\n\t"
    "buffer_load_dwordx4 v227, s[28:31], s80 offen offset:1024 nt lds\n\t"
    "v_add_f32 v211, v211, v154\n\t"
    "v_add_f32 v213, v213, v155\n\t"
    "v_max3_f32 v208, v208, v140, v141\n\t"
    "v_max3_f32 v209, v209, v156, v157\n\t"
    "v_fma_f32 v140, v140, s69, v214\n\t"
    "v_mfma_f32_32x32x16_bf16 v[176:191], a[144:147], a[100:103], v[176:191]\n\t"
    "v_fma_f32 v141, v141, s69, v214\n\t"
    "v_fma_f32 v156, v156, s69, v215\n\t"
    "v_fma_f32 v157, v157, s69, v215\n\t"
    "v_exp_f32 v140, v140\n\t"
    "v_mfma_f32_32x32x16_bf16 v[160:175], a[148:151], a[104:107], v[160:175]\n\t"
    "v_exp_f32 v141, v141\n\t"
    "v_exp_f32 v156, v156\n\t"
    "v_exp_f32 v157, v157\n\t"
    "v_cvt_pk_bf16_f32 v110, v140, v141\n\t"
    "v_add_f32 v210, v210, v140\n\t"
    "v_mfma_f32_32x32x16_bf16 v[176:191], a[148:151], a[108:111], v[176:191]\n\t"
    "buffer_load_dwordx4 v228, s[28:31], s80 offen offset:2048 nt lds\n\t"
    "v_add_f32 v212, v212, v141\n\t"
    "v_cvt_pk_bf16_f32 v198, v156, v157\n\t"
    "v_add_f32 v211, v211, v156\n\t"
    "v_add_f32 v213, v213, v157\n\t"
    "buffer_load_dwordx4 v229, s[28:31], s80 offen offset:3072 nt lds\n\t"
    "s_waitcnt vmcnt(12)\n\t"
    "ds_read_b128 a[128:131], v230 offset:8192\n\t"
    "ds_read_b128 a[132:135], v231 offset:8192\n\t"
    "v_mfma_f32_32x32x16_bf16 v[160:175], a[152:155], a[112:115], v[160:175]\n\t"
    "v_max3_f32 v208, v208, v142, v143\n\t"
    "v_max3_f32 v209, v209, v158, v159\n\t"
    "v_fma_f32 v142, v142, s69, v214\n\t"
    "v_fma_f32 v143, v143, s69, v214\n\t"
    "ds_read_b128 a[136:139], v232 offset:8192\n\t"
    "ds_read_b128 a[140:143], v233 offset:8192\n\t"
    "v_mfma_f32_32x32x16_bf16 v[176:191], a[152:155], a[116:119], v[176:191]\n\t"
    "v_fma_f32 v158, v158, s69, v215\n\t"
    "v_fma_f32 v159, v159, s69, v215\n\t"
    "v_exp_f32 v142, v142\n\t"
    "v_exp_f32 v143, v143\n\t"
    "v_exp_f32 v158, v158\n\t"
    "v_mfma_f32_32x32x16_bf16 v[160:175], a[156:159], a[120:123], v[160:175]\n\t"
    "s_add_u32 m0, s56, 0\n\t"
    "s_xor_b32 s80, s68, 155648\n\t"
    "buffer_load_dwordx4 v226, s[28:31], s80 offen nt lds\n\t"
    "v_exp_f32 v159, v159\n\t"
    "v_cvt_pk_bf16_f32 v111, v142, v143\n\t"
    "v_add_f32 v210, v210, v142\n\t"
    "v_add_f32 v212, v212, v143\n\t"
    "v_mfma_f32_32x32x16_bf16 v[176:191], a[156:159], a[124:127], v[176:191]\n\t"
    "ds_write_b128 v224, v[108:111] offset:33792\n\t"
    "v_cvt_pk_bf16_f32 v199, v158, v159\n\t"
    "v_add_f32 v211, v211, v158\n\t"
    "v_add_f32 v213, v213, v159\n\t"
    "ds_write_b128 v224, v[196:199] offset:35840\n\t"
    "s_waitcnt lgkmcnt(2)\n\t"
    "s_waitcnt lgkmcnt(0)\n\t"
    "s_barrier\n\t"
    "v_mfma_f32_32x32x16_bf16 v[128:143], a[128:131], a[0:3], 0\n\t"
    "buffer_load_dwordx4 v227, s[28:31], s80 offen offset:1024 nt lds\n\t"
    "ds_read_b128 a[184:187], v251 offset:34816\n\t"
    "ds_read_b128 a[192:195], v252 offset:32768\n\t"
    "v_mfma_f32_32x32x16_bf16 v[144:159], a[128:131], a[4:7], 0\n\t"
    "buffer_load_dwordx4 v228, s[28:31], s80 offen offset:2048 nt lds\n\t"
    "ds_read_b128 a[200:203], v253 offset:34816\n\t"
    "ds_read_b128 a[188:191], v251 offset:35840\n\t"
    "v_mfma_f32_32x32x16_bf16 v[128:143], a[132:135], a[8:11], v[128:143]\n\t"
    "v_max3_f32 v208, v208, v160, v161\n\t"
    "v_max3_f32 v209, v209, v176, v177\n\t"
    "v_fma_f32 v160, v160, s69, v214\n\t"
    "v_fma_f32 v161, v161, s69, v214\n\t"
    "v_mfma_f32_32x32x16_bf16 v[144:159], a[132:135], a[12:15], v[144:159]\n\t"
    "v_fma_f32 v176, v176, s69, v215\n\t"
    "v_fma_f32 v177, v177, s69, v215\n\t"
    "v_exp_f32 v160, v160\n\t"
    "v_exp_f32 v161, v161\n\t"
    "buffer_load_dwordx4 v229, s[28:31], s80 offen offset:3072 nt lds\n\t"
    "s_waitcnt vmcnt(12)\n\t"
    "ds_read_b128 a[144:147], v230 offset:12288\n\t"
    "ds_read_b128 a[148:151], v231 offset:12288\n\t"
    "v_mfma_f32_32x32x16_bf16 v[128:143], a[136:139], a[16:19], v[128:143]\n\t"
    "v_exp_f32 v176, v176\n\t"
    "v_exp_f32 v177, v177\n\t"
    "v_cvt_pk_bf16_f32 v112, v160, v161\n\t"
    "v_add_f32 v210, v210, v160\n\t"
    "v_add_f32 v212, v212, v161\n\t"
    "ds_read_b128 a[152:155], v232 offset:12288\n\t"
    "ds_read_b128 a[156:159], v233 offset:12288\n\t"
    "v_mfma_f32_32x32x16_bf16 v[144:159], a[136:139], a[20:23], v[144:159]\n\t"
    "ds_read_b128 a[196:199], v252 offset:33792\n\t"
    "ds_read_b128 a[204:207], v253 offset:35840\n\t"
    "v_cvt_pk_bf16_f32 v192, v176, v177\n\t"
    "v_add_f32 v211, v211, v176\n\t"
    "v_add_f32 v213, v213, v177\n\t"
    "v_max3_f32 v208, v208, v162, v163\n\t"
    "v_mfma_f32_32x32x16_bf16 v[128:143], a[140:143], a[24:27], v[128:143]\n\t"
    "s_add_u32 m0, s56, 4096\n\t"
    "s_xor_b32 s80, s68, 155776\n\t"
    "buffer_load_dwordx4 v226, s[28:31], s80 offen nt lds\n\t"
    "v_max3_f32 v209, v209, v178, v179\n\t"
    "v_fma_f32 v162, v162, s69, v214\n\t"
    "v_fma_f32 v163, v163, s69, v214\n\t"
    "v_fma_f32 v178, v178, s69, v215\n\t"
    "v_fma_f32 v179, v179, s69, v215\n\t"
    "v_mfma_f32_32x32x16_bf16 v[144:159], a[140:143], a[28:31], v[144:159]\n\t"
    "v_exp_f32 v162, v162\n\t"
    "v_exp_f32 v163, v163\n\t"
    "v_exp_f32 v178, v178\n\t"
    "v_exp_f32 v179, v179\n\t"
    "s_waitcnt lgkmcnt(2)\n\t"
    "v_mfma_f32_32x32x16_bf16 v[128:143], a[144:147], a[32:35], v[128:143]\n\t"
    "buffer_load_dwordx4 v227, s[28:31], s80 offen offset:1024 nt lds\n\t"
    "v_cvt_pk_bf16_f32 v113, v162, v163\n\t"
    "v_add_f32 v210, v210, v162\n\t"
    "v_add_f32 v212, v212, v163\n\t"
    "v_cvt_pk_bf16_f32 v193, v178, v179\n\t"
    "v_mfma_f32_32x32x16_bf16 v[144:159], a[144:147], a[36:39], v[144:159]\n\t"
    "v_add_f32 v211, v211, v178\n\t"
    "v_add_f32 v213, v213, v179\n\t"
    "v_max3_f32 v208, v208, v164, v165\n\t"
    "v_max3_f32 v209, v209, v180, v181\n\t"
    "v_fma_f32 v164, v164, s69, v214\n\t"
    "v_mfma_f32_32x32x16_bf16 v[128:143], a[148:151], a[40:43], v[128:143]\n\t"
    "v_fma_f32 v165, v165, s69, v214\n\t"
    "v_fma_f32 v180, v180, s69, v215\n\t"
    "v_fma_f32 v181, v181, s69, v215\n\t"
    "v_exp_f32 v164, v164\n\t"
    "v_mfma_f32_32x32x16_bf16 v[144:159], a[148:151], a[44:47], v[144:159]\n\t"
    "buffer_load_dwordx4 v228, s[28:31], s80 offen offset:2048 nt lds\n\t"
    "v_exp_f32 v165, v165\n\t"
    "v_exp_f32 v180, v180\n\t"
    "v_exp_f32 v181, v181\n\t"
    "v_cvt_pk_bf16_f32 v114, v164, v165\n\t"
    "v_add_f32 v210, v210, v164\n\t"
    "buffer_load_dwordx4 v229, s[28:31], s80 offen offset:3072 nt lds\n\t"
    "s_waitcnt vmcnt(12)\n\t"
    "ds_read_b128 a[128:131], v230 offset:16384\n\t"
    "ds_read_b128 a[132:135], v231 offset:16384\n\t"
    "v_mfma_f32_32x32x16_bf16 v[128:143], a[152:155], a[48:51], v[128:143]\n\t"
    "v_add_f32 v212, v212, v165\n\t"
    "v_cvt_pk_bf16_f32 v194, v180, v181\n\t"
    "v_add_f32 v211, v211, v180\n\t"
    "v_add_f32 v213, v213, v181\n\t"
    "ds_read_b128 a[136:139], v232 offset:16384\n\t"
    "ds_read_b128 a[140:143], v233 offset:16384\n\t"
    "v_mfma_f32_32x32x16_bf16 v[144:159], a[152:155], a[52:55], v[144:159]\n\t"
    "v_max3_f32 v208, v208, v166, v167\n\t"
    "v_max3_f32 v209, v209, v182, v183\n\t"
    "v_fma_f32 v166, v166, s69, v214\n\t"
    "v_fma_f32 v167, v167, s69, v214\n\t"
    "v_mfma_f32_32x32x16_bf16 v[128:143], a[156:159], a[56:59], v[128:143]\n\t"
    "s_add_u32 m0, s56, 24576\n\t"
    "s_xor_b32 s80, s68, 155904\n\t"
    "buffer_load_dwordx4 v226, s[28:31], s80 offen nt lds\n\t"
    "v_fma_f32 v182, v182, s69, v215\n\t"
    "v_fma_f32 v183, v183, s69, v215\n\t"
    "v_exp_f32 v166, v166\n\t"
    "v_exp_f32 v167, v167\n\t"
    "v_exp_f32 v182, v182\n\t"
    "v_mfma_f32_32x32x16_bf16 v[144:159], a[156:159], a[60:63], v[144:159]\n\t"
    "v_exp_f32 v183, v183\n\t"
    "v_cvt_pk_bf16_f32 v115, v166, v167\n\t"
    "v_add_f32 v210, v210, v166\n\t"
    "v_add_f32 v212, v212, v167\n\t"
    "s_waitcnt lgkmcnt(0)\n\t"
    "v_mfma_f32_32x32x16_bf16 v[128:143], a[128:131], a[64:67], v[128:143]\n\t"
    "buffer_load_dwordx4 v227, s[28:31], s80 offen offset:1024 nt lds\n\t"
    "ds_write_b128 v224, v[112:115] offset:36864\n\t"
    "v_cvt_pk_bf16_f32 v195, v182, v183\n\t"
    "v_add_f32 v211, v211, v182\n\t"
    "v_add_f32 v213, v213, v183\n\t"
    "ds_write_b128 v224, v[192:195] offset:38912\n\t"
    "v_mfma_f32_32x32x16_bf16 v[144:159], a[128:131], a[68:71], v[144:159]\n\t"
    "v_max3_f32 v208, v208, v168, v169\n\t"
    "v_max3_f32 v209, v209, v184, v185\n\t"
    "v_fma_f32 v168, v168, s69, v214\n\t"
    "v_fma_f32 v169, v169, s69, v214\n\t"
    "v_mfma_f32_32x32x16_bf16 v[128:143], a[132:135], a[72:75], v[128:143]\n\t"
    "v_fma_f32 v184, v184, s69, v215\n\t"
    "v_fma_f32 v185, v185, s69, v215\n\t"
    "v_exp_f32 v168, v168\n\t"
    "v_exp_f32 v169, v169\n\t"
    "v_mfma_f32_32x32x16_bf16 v[144:159], a[132:135], a[76:79], v[144:159]\n\t"
    "buffer_load_dwordx4 v228, s[28:31], s80 offen offset:2048 nt lds\n\t"
    "v_exp_f32 v184, v184\n\t"
    "v_exp_f32 v185, v185\n\t"
    "v_cvt_pk_bf16_f32 v116, v168, v169\n\t"
    "v_add_f32 v210, v210, v168\n\t"
    "v_add_f32 v212, v212, v169\n\t"
    "buffer_load_dwordx4 v229, s[28:31], s80 offen offset:3072 nt lds\n\t"
    "s_waitcnt vmcnt(12)\n\t"
    "ds_read_b128 a[144:147], v230 offset:20480\n\t"
    "ds_read_b128 a[148:151], v231 offset:20480\n\t"
    "v_mfma_f32_32x32x16_bf16 v[128:143], a[136:139], a[80:83], v[128:143]\n\t"
    "v_cvt_pk_bf16_f32 v196, v184, v185\n\t"
    "v_add_f32 v211, v211, v184\n\t"
    "v_add_f32 v213, v213, v185\n\t"
    "v_max3_f32 v208, v208, v170, v171\n\t"
    "ds_read_b128 a[152:155], v232 offset:20480\n\t"
    "ds_read_b128 a[156:159], v233 offset:20480\n\t"
    "v_mfma_f32_32x32x16_bf16 v[144:159], a[136:139], a[84:87], v[144:159]\n\t"
    "v_max3_f32 v209, v209, v186, v187\n\t"
    "v_fma_f32 v170, v170, s69, v214\n\t"
    "v_fma_f32 v171, v171, s69, v214\n\t"
    "v_fma_f32 v186, v186, s69, v215\n\t"
    "v_fma_f32 v187, v187, s69, v215\n\t"
    "v_mfma_f32_32x32x16_bf16 v[128:143], a[140:143], a[88:91], v[128:143]\n\t"
    "s_add_u32 m0, s56, 28672\n\t"
    "s_xor_b32 s80, s68, 156032\n\t"
    "buffer_load_dwordx4 v226, s[28:31], s80 offen nt lds\n\t"
    "v_exp_f32 v170, v170\n\t"
    "v_exp_f32 v171, v171\n\t"
    "v_exp_f32 v186, v186\n\t"
    "v_exp_f32 v187, v187\n\t"
    "v_mfma_f32_32x32x16_bf16 v[144:159], a[140:143], a[92:95], v[144:159]\n\t"
    "v_cvt_pk_bf16_f32 v117, v170, v171\n\t"
    "v_add_f32 v210, v210, v170\n\t"
    "v_add_f32 v212, v212, v171\n\t"
    "v_cvt_pk_bf16_f32 v197, v186, v187\n\t"
    "s_waitcnt lgkmcnt(0)\n\t"
    "v_mfma_f32_32x32x16_bf16 v[128:143], a[144:147], a[96:99], v[128:143]\n\t"
    "buffer_load_dwordx4 v227, s[28:31], s80 offen offset:1024 nt lds\n\t"
    "v_add_f32 v211, v211, v186\n\t"
    "v_add_f32 v213, v213, v187\n\t"
    "v_max3_f32 v208, v208, v172, v173\n\t"
    "v_max3_f32 v209, v209, v188, v189\n\t"
    "v_fma_f32 v172, v172, s69, v214\n\t"
    "v_mfma_f32_32x32x16_bf16 v[144:159], a[144:147], a[100:103], v[144:159]\n\t"
    "v_fma_f32 v173, v173, s69, v214\n\t"
    "v_fma_f32 v188, v188, s69, v215\n\t"
    "v_fma_f32 v189, v189, s69, v215\n\t"
    "v_exp_f32 v172, v172\n\t"
    "v_mfma_f32_32x32x16_bf16 v[128:143], a[148:151], a[104:107], v[128:143]\n\t"
    "v_exp_f32 v173, v173\n\t"
    "v_exp_f32 v188, v188\n\t"
    "v_exp_f32 v189, v189\n\t"
    "v_cvt_pk_bf16_f32 v118, v172, v173\n\t"
    "v_add_f32 v210, v210, v172\n\t"
    "v_mfma_f32_32x32x16_bf16 v[144:159], a[148:151], a[108:111], v[144:159]\n\t"
    "buffer_load_dwordx4 v228, s[28:31], s80 offen offset:2048 nt lds\n\t"
    "v_add_f32 v212, v212, v173\n\t"
    "v_cvt_pk_bf16_f32 v198, v188, v189\n\t"
    "v_add_f32 v211, v211, v188\n\t"
    "v_add_f32 v213, v213, v189\n\t"
    "buffer_load_dwordx4 v229, s[28:31], s80 offen offset:3072 nt lds\n\t"
    "s_waitcnt vmcnt(12)\n\t"
    "ds_read_b128 a[128:131], v230 offset:0\n\t"
    "ds_read_b128 a[132:135], v231 offset:0\n\t"
    "v_mfma_f32_32x32x16_bf16 v[128:143], a[152:155], a[112:115], v[128:143]\n\t"
    "v_max3_f32 v208, v208, v174, v175\n\t"
    "v_max3_f32 v209, v209, v190, v191\n\t"
    "v_fma_f32 v174, v174, s69, v214\n\t"
    "v_fma_f32 v175, v175, s69, v214\n\t"
    "ds_read_b128 a[136:139], v232 offset:0\n\t"
    "ds_read_b128 a[140:143], v233 offset:0\n\t"
    "v_mfma_f32_32x32x16_bf16 v[144:159], a[152:155], a[116:119], v[144:159]\n\t"
    "v_fma_f32 v190, v190, s69, v215\n\t"
    "v_fma_f32 v191, v191, s69, v215\n\t"
    "v_exp_f32 v174, v174\n\t"
    "v_exp_f32 v175, v175\n\t"
    "v_exp_f32 v190, v190\n\t"
    "v_mfma_f32_32x32x16_bf16 v[128:143], a[156:159], a[120:123], v[128:143]\n\t"
    "s_add_u32 m0, s56, 8192\n\t"
    "s_xor_b32 s80, s64, 0\n\t"
    "buffer_load_dwordx4 v240, s[32:35], s80 offen nt lds\n\t"
    "v_exp_f32 v191, v191\n\t"
    "v_cvt_pk_bf16_f32 v119, v174, v175\n\t"
    "v_add_f32 v210, v210, v174\n\t"
    "v_add_f32 v212, v212, v175\n\t"
    "v_mfma_f32_32x32x16_bf16 v[144:159], a[156:159], a[124:127], v[144:159]\n\t"
    "ds_write_b128 v224, v[116:119] offset:37888\n\t"
    "v_cvt_pk_bf16_f32 v199, v190, v191\n\t"
    "v_add_f32 v211, v211, v190\n\t"
    "v_add_f32 v213, v213, v191\n\t"
    "ds_write_b128 v224, v[196:199] offset:39936\n\t"
    "s_waitcnt lgkmcnt(2)\n\t"
    "s_waitcnt lgkmcnt(0)\n\t"
    "s_barrier\n\t"
    "v_mfma_f32_32x32x16_bf16 v[160:175], a[128:131], a[0:3], 0\n\t"
    "buffer_load_dwordx4 v241, s[32:35], s80 offen offset:1024 nt lds\n\t"
    "ds_read_b128 a[208:211], v251 offset:38912\n\t"
    "ds_read_b128 a[216:219], v252 offset:36864\n\t"
    "v_mfma_f32_32x32x16_bf16 v[176:191], a[128:131], a[4:7], 0\n\t"
    "buffer_load_dwordx4 v242, s[32:35], s80 offen offset:2048 nt lds\n\t"
    "ds_read_b128 a[224:227], v253 offset:38912\n\t"
    "ds_read_b128 a[212:215], v251 offset:39936\n\t"
    "v_mfma_f32_32x32x16_bf16 v[160:175], a[132:135], a[8:11], v[160:175]\n\t"
    "v_max3_f32 v208, v208, v128, v129\n\t"
    "v_max3_f32 v209, v209, v144, v145\n\t"
    "v_fma_f32 v128, v128, s69, v214\n\t"
    "v_fma_f32 v129, v129, s69, v214\n\t"
    "v_mfma_f32_32x32x16_bf16 v[176:191], a[132:135], a[12:15], v[176:191]\n\t"
    "v_fma_f32 v144, v144, s69, v215\n\t"
    "v_fma_f32 v145, v145, s69, v215\n\t"
    "v_exp_f32 v128, v128\n\t"
    "v_exp_f32 v129, v129\n\t"
    "buffer_load_dwordx4 v243, s[32:35], s80 offen offset:3072 nt lds\n\t"
    "s_waitcnt vmcnt(12)\n\t"
    "ds_read_b128 a[144:147], v230 offset:4096\n\t"
    "ds_read_b128 a[148:151], v231 offset:4096\n\t"
    "v_mfma_f32_32x32x16_bf16 v[160:175], a[136:139], a[16:19], v[160:175]\n\t"
    "v_exp_f32 v144, v144\n\t"
    "v_exp_f32 v145, v145\n\t"
    "v_cvt_pk_bf16_f32 v120, v128, v129\n\t"
    "v_add_f32 v210, v210, v128\n\t"
    "v_add_f32 v212, v212, v129\n\t"
    "ds_read_b128 a[152:155], v232 offset:4096\n\t"
    "ds_read_b128 a[156:159], v233 offset:4096\n\t"
    "v_mfma_f32_32x32x16_bf16 v[176:191], a[136:139], a[20:23], v[176:191]\n\t"
    "ds_read_b128 a[220:223], v252 offset:37888\n\t"
    "ds_read_b128 a[228:231], v253 offset:39936\n\t"
    "v_cvt_pk_bf16_f32 v192, v144, v145\n\t"
    "v_add_f32 v211, v211, v144\n\t"
    "v_add_f32 v213, v213, v145\n\t"
    "v_max3_f32 v208, v208, v130, v131\n\t"
    "v_mfma_f32_32x32x16_bf16 v[160:175], a[140:143], a[24:27], v[160:175]\n\t"
    "s_add_u32 m0, s56, 12288\n\t"
    "s_xor_b32 s80, s64, 131072\n\t"
    "buffer_load_dwordx4 v240, s[32:35], s80 offen nt lds\n\t"
    "v_max3_f32 v209, v209, v146, v147\n\t"
    "v_fma_f32 v130, v130, s69, v214\n\t"
    "v_fma_f32 v131, v131, s69, v214\n\t"
    "v_fma_f32 v146, v146, s69, v215\n\t"
    "v_fma_f32 v147, v147, s69, v215\n\t"
    "v_mfma_f32_32x32x16_bf16 v[176:191], a[140:143], a[28:31], v[176:191]\n\t"
    "v_exp_f32 v130, v130\n\t"
    "v_exp_f32 v131, v131\n\t"
    "v_exp_f32 v146, v146\n\t"
    "v_exp_f32 v147, v147\n\t"
    "s_waitcnt lgkmcnt(2)\n\t"
    "v_mfma_f32_32x32x16_bf16 v[160:175], a[144:147], a[32:35], v[160:175]\n\t"
    "buffer_load_dwordx4 v241, s[32:35], s80 offen offset:1024 nt lds\n\t"
    "v_cvt_pk_bf16_f32 v121, v130, v131\n\t"
    "v_add_f32 v210, v210, v130\n\t"
    "v_add_f32 v212, v212, v131\n\t"
    "v_cvt_pk_bf16_f32 v193, v146, v147\n\t"
    "v_mfma_f32_32x32x16_bf16 v[176:191], a[144:147], a[36:39], v[176:191]\n\t"
    "v_add_f32 v211, v211, v146\n\t"
    "v_add_f32 v213, v213, v147\n\t"
    "v_max3_f32 v208, v208, v132, v133\n\t"
    "v_max3_f32 v209, v209, v148, v149\n\t"
    "v_fma_f32 v132, v132, s69, v214\n\t"
    "v_mfma_f32_32x32x16_bf16 v[160:175], a[148:151], a[40:43], v[160:175]\n\t"
    "v_fma_f32 v133, v133, s69, v214\n\t"
    "v_fma_f32 v148, v148, s69, v215\n\t"
    "v_fma_f32 v149, v149, s69, v215\n\t"
    "v_exp_f32 v132, v132\n\t"
    "v_mfma_f32_32x32x16_bf16 v[176:191], a[148:151], a[44:47], v[176:191]\n\t"
    "buffer_load_dwordx4 v242, s[32:35], s80 offen offset:2048 nt lds\n\t"
    "v_exp_f32 v133, v133\n\t"
    "v_exp_f32 v148, v148\n\t"
    "v_exp_f32 v149, v149\n\t"
    "v_cvt_pk_bf16_f32 v122, v132, v133\n\t"
    "v_add_f32 v210, v210, v132\n\t"
    "buffer_load_dwordx4 v243, s[32:35], s80 offen offset:3072 nt lds\n\t"
    "s_waitcnt vmcnt(12)\n\t"
    "ds_read_b128 a[128:131], v230 offset:24576\n\t"
    "ds_read_b128 a[132:135], v231 offset:24576\n\t"
    "v_mfma_f32_32x32x16_bf16 v[160:175], a[152:155], a[48:51], v[160:175]\n\t"
    "v_add_f32 v212, v212, v133\n\t"
    "v_cvt_pk_bf16_f32 v194, v148, v149\n\t"
    "v_add_f32 v211, v211, v148\n\t"
    "v_add_f32 v213, v213, v149\n\t"
    "ds_read_b128 a[136:139], v232 offset:24576\n\t"
    "ds_read_b128 a[140:143], v233 offset:24576\n\t"
    "v_mfma_f32_32x32x16_bf16 v[176:191], a[152:155], a[52:55], v[176:191]\n\t"
    "v_max3_f32 v208, v208, v134, v135\n\t"
    "v_max3_f32 v209, v209, v150, v151\n\t"
    "v_fma_f32 v134, v134, s69, v214\n\t"
    "v_fma_f32 v135, v135, s69, v214\n\t"
    "v_mfma_f32_32x32x16_bf16 v[160:175], a[156:159], a[56:59], v[160:175]\n\t"
    "s_add_u32 m0, s56, 16384\n\t"
    "s_xor_b32 s80, s64, 8192\n\t"
    "buffer_load_dwordx4 v240, s[32:35], s80 offen nt lds\n\t"
    "v_fma_f32 v150, v150, s69, v215\n\t"
    "v_fma_f32 v151, v151, s69, v215\n\t"
    "v_exp_f32 v134, v134\n\t"
    "v_exp_f32 v135, v135\n\t"
    "v_exp_f32 v150, v150\n\t"
    "v_mfma_f32_32x32x16_bf16 v[176:191], a[156:159], a[60:63], v[176:191]\n\t"
    "v_exp_f32 v151, v151\n\t"
    "v_cvt_pk_bf16_f32 v123, v134, v135\n\t"
    "v_add_f32 v210, v210, v134\n\t"
    "v_add_f32 v212, v212, v135\n\t"
    "s_waitcnt lgkmcnt(0)\n\t"
    "v_mfma_f32_32x32x16_bf16 v[160:175], a[128:131], a[64:67], v[160:175]\n\t"
    "buffer_load_dwordx4 v241, s[32:35], s80 offen offset:1024 nt lds\n\t"
    "ds_write_b128 v224, v[120:123] offset:32768\n\t"
    "v_cvt_pk_bf16_f32 v195, v150, v151\n\t"
    "v_add_f32 v211, v211, v150\n\t"
    "v_add_f32 v213, v213, v151\n\t"
    "ds_write_b128 v224, v[192:195] offset:34816\n\t"
    "v_mfma_f32_32x32x16_bf16 v[176:191], a[128:131], a[68:71], v[176:191]\n\t"
    "v_max3_f32 v208, v208, v136, v137\n\t"
    "v_max3_f32 v209, v209, v152, v153\n\t"
    "v_fma_f32 v136, v136, s69, v214\n\t"
    "v_fma_f32 v137, v137, s69, v214\n\t"
    "v_mfma_f32_32x32x16_bf16 v[160:175], a[132:135], a[72:75], v[160:175]\n\t"
    "v_fma_f32 v152, v152, s69, v215\n\t"
    "v_fma_f32 v153, v153, s69, v215\n\t"
    "v_exp_f32 v136, v136\n\t"
    "v_exp_f32 v137, v137\n\t"
    "v_mfma_f32_32x32x16_bf16 v[176:191], a[132:135], a[76:79], v[176:191]\n\t"
    "buffer_load_dwordx4 v242, s[32:35], s80 offen offset:2048 nt lds\n\t"
    "v_exp_f32 v152, v152\n\t"
    "v_exp_f32 v153, v153\n\t"
    "v_cvt_pk_bf16_f32 v124, v136, v137\n\t"
    "v_add_f32 v210, v210, v136\n\t"
    "v_add_f32 v212, v212, v137\n\t"
    "buffer_load_dwordx4 v243, s[32:35], s80 offen offset:3072 nt lds\n\t"
    "s_waitcnt vmcnt(12)\n\t"
    "ds_read_b128 a[144:147], v230 offset:28672\n\t"
    "ds_read_b128 a[148:151], v231 offset:28672\n\t"
    "v_mfma_f32_32x32x16_bf16 v[160:175], a[136:139], a[80:83], v[160:175]\n\t"
    "v_cvt_pk_bf16_f32 v196, v152, v153\n\t"
    "v_add_f32 v211, v211, v152\n\t"
    "v_add_f32 v213, v213, v153\n\t"
    "v_max3_f32 v208, v208, v138, v139\n\t"
    "ds_read_b128 a[152:155], v232 offset:28672\n\t"
    "ds_read_b128 a[156:159], v233 offset:28672\n\t"
    "v_mfma_f32_32x32x16_bf16 v[176:191], a[136:139], a[84:87], v[176:191]\n\t"
    "v_max3_f32 v209, v209, v154, v155\n\t"
    "v_fma_f32 v138, v138, s69, v214\n\t"
    "v_fma_f32 v139, v139, s69, v214\n\t"
    "v_fma_f32 v154, v154, s69, v215\n\t"
    "v_fma_f32 v155, v155, s69, v215\n\t"
    "v_mfma_f32_32x32x16_bf16 v[160:175], a[140:143], a[88:91], v[160:175]\n\t"
    "s_add_u32 m0, s56, 20480\n\t"
    "s_xor_b32 s80, s64, 139264\n\t"
    "buffer_load_dwordx4 v240, s[32:35], s80 offen nt lds\n\t"
    "v_exp_f32 v138, v138\n\t"
    "v_exp_f32 v139, v139\n\t"
    "v_exp_f32 v154, v154\n\t"
    "v_exp_f32 v155, v155\n\t"
    "v_mfma_f32_32x32x16_bf16 v[176:191], a[140:143], a[92:95], v[176:191]\n\t"
    "v_cvt_pk_bf16_f32 v125, v138, v139\n\t"
    "v_add_f32 v210, v210, v138\n\t"
    "v_add_f32 v212, v212, v139\n\t"
    "v_cvt_pk_bf16_f32 v197, v154, v155\n\t"
    "s_waitcnt lgkmcnt(0)\n\t"
    "v_mfma_f32_32x32x16_bf16 v[160:175], a[144:147], a[96:99], v[160:175]\n\t"
    "buffer_load_dwordx4 v241, s[32:35], s80 offen offset:1024 nt lds\n\t"
    "v_add_f32 v211, v211, v154\n\t"
    "v_add_f32 v213, v213, v155\n\t"
    "v_max3_f32 v208, v208, v140, v141\n\t"
    "v_max3_f32 v209, v209, v156, v157\n\t"
    "v_fma_f32 v140, v140, s69, v214\n\t"
    "v_mfma_f32_32x32x16_bf16 v[176:191], a[144:147], a[100:103], v[176:191]\n\t"
    "v_fma_f32 v141, v141, s69, v214\n\t"
    "v_fma_f32 v156, v156, s69, v215\n\t"
    "v_fma_f32 v157, v157, s69, v215\n\t"
    "v_exp_f32 v140, v140\n\t"
    "v_mfma_f32_32x32x16_bf16 v[160:175], a[148:151], a[104:107], v[160:175]\n\t"
    "v_exp_f32 v141, v141\n\t"
    "v_exp_f32 v156, v156\n\t"
    "v_exp_f32 v157, v157\n\t"
    "v_cvt_pk_bf16_f32 v126, v140, v141\n\t"
    "v_add_f32 v210, v210, v140\n\t"
    "buffer_load_dwordx4 v242, s[32:35], s80 offen offset:2048 nt lds\n\t"
    "buffer_load_dwordx4 v243, s[32:35], s80 offen offset:3072 nt lds\n\t"
    "s_waitcnt vmcnt(12)\n\t"
    "s_barrier\n\t"
    "ds_read_b64_tr_b16 a[32:33], v244 offset:8192\n\t"
    "ds_read_b64_tr_b16 a[34:35], v244 offset:9216\n\t"
    "ds_read_b64_tr_b16 a[40:41], v234 offset:8192\n\t"
    "v_mfma_f32_32x32x16_bf16 v[176:191], a[148:151], a[108:111], v[176:191]\n\t"
    "v_add_f32 v212, v212, v141\n\t"
    "v_cvt_pk_bf16_f32 v198, v156, v157\n\t"
    "v_add_f32 v211, v211, v156\n\t"
    "v_add_f32 v213, v213, v157\n\t"
    "ds_read_b64_tr_b16 a[42:43], v234 offset:9216\n\t"
    "ds_read_b64_tr_b16 a[48:49], v235 offset:8192\n\t"
    "ds_read_b64_tr_b16 a[50:51], v235 offset:9216\n\t"
    "v_mfma_f32_32x32x16_bf16 v[160:175], a[152:155], a[112:115], v[160:175]\n\t"
    "s_add_u32 m0, s56, 0\n\t"
    "s_xor_b32 s80, s64, 16384\n\t"
    "buffer_load_dwordx4 v240, s[32:35], s80 offen nt lds\n\t"
    "v_max3_f32 v208, v208, v142, v143\n\t"
    "v_max3_f32 v209, v209, v158, v159\n\t"
    "v_fma_f32 v142, v142, s69, v214\n\t"
    "v_fma_f32 v143, v143, s69, v214\n\t"
    "ds_read_b64_tr_b16 a[56:57], v236 offset:8192\n\t"
    "ds_read_b64_tr_b16 a[58:59], v236 offset:9216\n\t"
    "ds_read_b64_tr_b16 a[36:37], v244 offset:10240\n\t"
    "ds_read_b64_tr_b16 a[38:39], v244 offset:11264\n\t"
    "v_mfma_f32_32x32x16_bf16 v[176:191], a[152:155], a[116:119], v[176:191]\n\t"
    "buffer_load_dwordx4 v241, s[32:35], s80 offen offset:1024 nt lds\n\t"
    "v_fma_f32 v158, v158, s69, v215\n\t"
    "v_fma_f32 v159, v159, s69, v215\n\t"
    "v_exp_f32 v142, v142\n\t"
    "v_exp_f32 v143, v143\n\t"
    "v_exp_f32 v158, v158\n\t"
    "ds_read_b64_tr_b16 a[44:45], v234 offset:10240\n\t"
    "ds_read_b64_tr_b16 a[46:47], v234 offset:11264\n\t"
    "ds_read_b64_tr_b16 a[52:53], v235 offset:10240\n\t"
    "v_mfma_f32_32x32x16_bf16 v[160:175], a[156:159], a[120:123], v[160:175]\n\t"
    "v_exp_f32 v159, v159\n\t"
    "v_cvt_pk_bf16_f32 v127, v142, v143\n\t"
    "v_add_f32 v210, v210, v142\n\t"
    "v_add_f32 v212, v212, v143\n\t"
    "ds_read_b64_tr_b16 a[54:55], v235 offset:11264\n\t"
    "ds_read_b64_tr_b16 a[60:61], v236 offset:10240\n\t"
    "ds_read_b64_tr_b16 a[62:63], v236 offset:11264\n\t"
    "v_mfma_f32_32x32x16_bf16 v[176:191], a[156:159], a[124:127], v[176:191]\n\t"
    "buffer_load_dwordx4 v242, s[32:35], s80 offen offset:2048 nt lds\n\t"
    "ds_write_b128 v224, v[124:127] offset:33792\n\t"
    "v_cvt_pk_bf16_f32 v199, v158, v159\n\t"
    "v_add_f32 v211, v211, v158\n\t"
    "v_add_f32 v213, v213, v159\n\t"
    "ds_write_b128 v224, v[196:199] offset:35840\n\t"
    "s_waitcnt lgkmcnt(14)\n\t"
    "v_mfma_f32_32x32x16_bf16 a[96:111], a[32:35], v[0:3], 0\n\t"
    "v_mfma_f32_32x32x16_bf16 a[96:111], a[40:43], v[8:11], a[96:111]\n\t"
    "s_waitcnt lgkmcnt(12)\n\t"
    "v_mfma_f32_32x32x16_bf16 a[96:111], a[48:51], v[16:19], a[96:111]\n\t"
    "v_max3_f32 v208, v208, v160, v161\n\t"
    "v_max3_f32 v209, v209, v176, v177\n\t"
    "v_fma_f32 v160, v160, s69, v214\n\t"
    "v_fma_f32 v161, v161, s69, v214\n\t"
    "s_waitcnt lgkmcnt(10)\n\t"
    "v_mfma_f32_32x32x16_bf16 a[96:111], a[56:59], v[24:27], a[96:111]\n\t"
    "v_fma_f32 v176, v176, s69, v215\n\t"
    "v_fma_f32 v177, v177, s69, v215\n\t"
    "v_exp_f32 v160, v160\n\t"
    "v_exp_f32 v161, v161\n\t"
    "v_exp_f32 v176, v176\n\t"
    "buffer_load_dwordx4 v243, s[32:35], s80 offen offset:3072 nt lds\n\t"
    "s_waitcnt vmcnt(12)\n\t"
    "s_waitcnt lgkmcnt(0)\n\t"
    "s_barrier\n\t"
    "ds_read_b64_tr_b16 v[128:129], v244 offset:12288\n\t"
    "ds_read_b64_tr_b16 v[130:131], v244 offset:13312\n\t"
    "ds_read_b64_tr_b16 v[136:137], v234 offset:12288\n\t"
    "v_mfma_f32_32x32x16_bf16 a[96:111], a[36:39], v[4:7], a[96:111]\n\t"
    "v_exp_f32 v177, v177\n\t"
    "v_cvt_pk_bf16_f32 v200, v160, v161\n\t"
    "v_add_f32 v210, v210, v160\n\t"
    "v_add_f32 v212, v212, v161\n\t"
    "v_cvt_pk_bf16_f32 v192, v176, v177\n\t"
    "ds_read_b64_tr_b16 v[138:139], v234 offset:13312\n\t"
    "ds_read_b64_tr_b16 v[144:145], v235 offset:12288\n\t"
    "ds_read_b64_tr_b16 v[146:147], v235 offset:13312\n\t"
    "v_mfma_f32_32x32x16_bf16 a[96:111], a[44:47], v[12:15], a[96:111]\n\t"
    "s_add_u32 m0, s56, 4096\n\t"
    "s_xor_b32 s80, s64, 147456\n\t"
    "buffer_load_dwordx4 v240, s[32:35], s80 offen nt lds\n\t"
    "v_add_f32 v211, v211, v176\n\t"
    "v_add_f32 v213, v213, v177\n\t"
    "v_max3_f32 v208, v208, v162, v163\n\t"
    "v_max3_f32 v209, v209, v178, v179\n\t"
    "v_fma_f32 v162, v162, s69, v214\n\t"
    "ds_read_b64_tr_b16 v[152:153], v236 offset:12288\n\t"
    "ds_read_b64_tr_b16 v[154:155], v236 offset:13312\n\t"
    "ds_read_b64_tr_b16 v[132:133], v244 offset:14336\n\t"
    "ds_read_b64_tr_b16 v[134:135], v244 offset:15360\n\t"
    "v_mfma_f32_32x32x16_bf16 a[96:111], a[52:55], v[20:23], a[96:111]\n\t"
    "v_fma_f32 v163, v163, s69, v214\n\t"
    "v_fma_f32 v178, v178, s69, v215\n\t"
    "v_fma_f32 v179, v179, s69, v215\n\t"
    "v_exp_f32 v162, v162\n\t"
    "v_exp_f32 v163, v163\n\t"
    "ds_read_b64_tr_b16 v[140:141], v234 offset:14336\n\t"
    "ds_read_b64_tr_b16 v[142:143], v234 offset:15360\n\t"
    "ds_read_b64_tr_b16 v[148:149], v235 offset:14336\n\t"
    "v_mfma_f32_32x32x16_bf16 a[96:111], a[60:63], v[28:31], a[96:111]\n\t"
    "buffer_load_dwordx4 v241, s[32:35], s80 offen offset:1024 nt lds\n\t"
    "v_exp_f32 v178, v178\n\t"
    "v_exp_f32 v179, v179\n\t"
    "v_cvt_pk_bf16_f32 v201, v162, v163\n\t"
    "v_add_f32 v210, v210, v162\n\t"
    "v_add_f32 v212, v212, v163\n\t"
    "ds_read_b64_tr_b16 v[150:151], v235 offset:15360\n\t"
    "ds_read_b64_tr_b16 v[156:157], v236 offset:14336\n\t"
    "ds_read_b64_tr_b16 v[158:159], v236 offset:15360\n\t"
    "s_waitcnt lgkmcnt(14)\n\t"
    "v_mfma_f32_32x32x16_bf16 a[96:111], v[128:131], v[64:67], a[96:111]\n\t"
    "ds_read_b128 a[232:235], v251 offset:34816\n\t"
    "ds_read_b128 a[240:243], v252 offset:32768\n\t"
    "ds_read_b128 a[248:251], v253 offset:34816\n\t"
    "v_cvt_pk_bf16_f32 v193, v178, v179\n\t"
    "v_add_f32 v211, v211, v178\n\t"
    "v_add_f32 v213, v213, v179\n\t"
    "v_max3_f32 v208, v208, v164, v165\n\t"
    "v_max3_f32 v209, v209, v180, v181\n\t"
    "s_waitcnt lgkmcnt(14)\n\t"
    "v_mfma_f32_32x32x16_bf16 a[96:111], v[136:139], v[72:75], a[96:111]\n\t"
    "buffer_load_dwordx4 v242, s[32:35], s80 offen offset:2048 nt lds\n\t"
    "ds_read_b128 a[236:239], v251 offset:35840\n\t"
    "ds_read_b128 a[244:247], v252 offset:33792\n\t"
    "ds_read_b128 a[252:255], v253 offset:35840\n\t"
    "v_fma_f32 v164, v164, s69, v214\n\t"
    "v_fma_f32 v165, v165, s69, v214\n\t"
    "v_fma_f32 v180, v180, s69, v215\n\t"
    "v_fma_f32 v181, v181, s69, v215\n\t"
    "v_exp_f32 v164, v164\n\t"
    "s_waitcnt lgkmcnt(14)\n\t"
    "v_mfma_f32_32x32x16_bf16 a[96:111], v[144:147], v[80:83], a[96:111]\n\t"
    "buffer_load_dwordx4 v243, s[32:35], s80 offen offset:3072 nt lds\n\t"
    "v_exp_f32 v165, v165\n\t"
    "v_exp_f32 v180, v180\n\t"
    "v_exp_f32 v181, v181\n\t"
    "v_cvt_pk_bf16_f32 v202, v164, v165\n\t"
    "v_add_f32 v210, v210, v164\n\t"
    "v_mfma_f32_32x32x16_bf16 a[96:111], v[152:155], v[88:91], a[96:111]\n\t"
    "v_add_f32 v212, v212, v165\n\t"
    "v_cvt_pk_bf16_f32 v194, v180, v181\n\t"
    "v_add_f32 v211, v211, v180\n\t"
    "v_add_f32 v213, v213, v181\n\t"
    "v_max3_f32 v208, v208, v166, v167\n\t"
    "s_waitcnt vmcnt(12)\n\t"
    "s_waitcnt lgkmcnt(0)\n\t"
    "s_barrier\n\t"
    "ds_read_b64_tr_b16 a[128:129], v244 offset:16384\n\t"
    "ds_read_b64_tr_b16 a[130:131], v244 offset:17408\n\t"
    "ds_read_b64_tr_b16 a[136:137], v234 offset:16384\n\t"
    "v_mfma_f32_32x32x16_bf16 a[96:111], v[132:135], v[68:71], a[96:111]\n\t"
    "s_add_u32 m0, s56, 24576\n\t"
    "s_xor_b32 s80, s64, 24576\n\t"
    "buffer_load_dwordx4 v240, s[32:35], s80 offen nt lds\n\t"
    "v_max3_f32 v209, v209, v182, v183\n\t"
    "v_fma_f32 v166, v166, s69, v214\n\t"
    "v_fma_f32 v167, v167, s69, v214\n\t"
    "v_fma_f32 v182, v182, s69, v215\n\t"
    "ds_read_b64_tr_b16 a[138:139], v234 offset:17408\n\t"
    "ds_read_b64_tr_b16 a[144:145], v235 offset:16384\n\t"
    "ds_read_b64_tr_b16 a[146:147], v235 offset:17408\n\t"
    "v_mfma_f32_32x32x16_bf16 a[96:111], v[140:143], v[76:79], a[96:111]\n\t"
    "v_fma_f32 v183, v183, s69, v215\n\t"
    "v_exp_f32 v166, v166\n\t"
    "v_exp_f32 v167, v167\n\t"
    "v_exp_f32 v182, v182\n\t"
    "v_exp_f32 v183, v183\n\t"
    "ds_read_b64_tr_b16 a[152:153], v236 offset:16384\n\t"
    "ds_read_b64_tr_b16 a[154:155], v236 offset:17408\n\t"
    "ds_read_b64_tr_b16 a[132:133], v244 offset:18432\n\t"
    "ds_read_b64_tr_b16 a[134:135], v244 offset:19456\n\t"
    "v_mfma_f32_32x32x16_bf16 a[96:111], v[148:151], v[84:87], a[96:111]\n\t"
    "buffer_load_dwordx4 v241, s[32:35], s80 offen offset:1024 nt lds\n\t"
    "v_cvt_pk_bf16_f32 v203, v166, v167\n\t"
    "v_add_f32 v210, v210, v166\n\t"
    "v_add_f32 v212, v212, v167\n\t"
    "ds_write_b128 v224, v[200:203] offset:36864\n\t"
    "v_cvt_pk_bf16_f32 v195, v182, v183\n\t"
    "ds_read_b64_tr_b16 a[140:141], v234 offset:18432\n\t"
    "ds_read_b64_tr_b16 a[142:143], v234 offset:19456\n\t"
    "ds_read_b64_tr_b16 a[148:149], v235 offset:18432\n\t"
    "v_mfma_f32_32x32x16_bf16 a[96:111], v[156:159], v[92:95], a[96:111]\n\t"
    "buffer_load_dwordx4 v242, s[32:35], s80 offen offset:2048 nt lds\n\t"
    "v_add_f32 v211, v211, v182\n\t"
    "v_add_f32 v213, v213, v183\n\t"
    "ds_write_b128 v224, v[192:195] offset:38912\n\t"
    "v_max3_f32 v208, v208, v168, v169\n\t"
    "v_max3_f32 v209, v209, v184, v185\n\t"
    "ds_read_b64_tr_b16 a[150:151], v235 offset:19456\n\t"
    "ds_read_b64_tr_b16 a[156:157], v236 offset:18432\n\t"
    "ds_read_b64_tr_b16 a[158:159], v236 offset:19456\n\t"
    "s_waitcnt lgkmcnt(14)\n\t"
    "v_mfma_f32_32x32x16_bf16 a[96:111], a[128:131], v[32:35], a[96:111]\n\t"
    "v_fma_f32 v168, v168, s69, v214\n\t"
    "v_fma_f32 v169, v169, s69, v214\n\t"
    "v_fma_f32 v184, v184, s69, v215\n\t"
    "v_fma_f32 v185, v185, s69, v215\n\t"
    "v_exp_f32 v168, v168\n\t"
    "v_mfma_f32_32x32x16_bf16 a[96:111], a[136:139], v[40:43], a[96:111]\n\t"
    "v_exp_f32 v169, v169\n\t"
    "v_exp_f32 v184, v184\n\t"
    "v_exp_f32 v185, v185\n\t"
    "v_cvt_pk_bf16_f32 v204, v168, v169\n\t"
    "v_add_f32 v210, v210, v168\n\t"
    "s_waitcnt lgkmcnt(12)\n\t"
    "v_mfma_f32_32x32x16_bf16 a[96:111], a[144:147], v[48:51], a[96:111]\n\t"
    "v_add_f32 v212, v212, v169\n\t"
    "v_cvt_pk_bf16_f32 v196, v184, v185\n\t"
    "v_add_f32 v211, v211, v184\n\t"
    "v_add_f32 v213, v213, v185\n\t"
    "v_max3_f32 v208, v208, v170, v171\n\t"
    "s_waitcnt lgkmcnt(10)\n\t"
    "v_mfma_f32_32x32x16_bf16 a[96:111], a[152:155], v[56:59], a[96:111]\n\t"
    "v_max3_f32 v209, v209, v186, v187\n\t"
    "v_fma_f32 v170, v170, s69, v214\n\t"
    "v_fma_f32 v171, v171, s69, v214\n\t"
    "v_fma_f32 v186, v186, s69, v215\n\t"
    "v_fma_f32 v187, v187, s69, v215\n\t"
    "buffer_load_dwordx4 v243, s[32:35], s80 offen offset:3072 nt lds\n\t"
    "s_waitcnt vmcnt(12)\n\t"
    "s_waitcnt lgkmcnt(0)\n\t"
    "s_barrier\n\t"
    "ds_read_b64_tr_b16 v[128:129], v244 offset:20480\n\t"
    "ds_read_b64_tr_b16 v[130:131], v244 offset:21504\n\t"
    "ds_read_b64_tr_b16 v[136:137], v234 offset:20480\n\t"
    "v_mfma_f32_32x32x16_bf16 a[96:111], a[132:135], v[36:39], a[96:111]\n\t"
    "v_exp_f32 v170, v170\n\t"
    "v_exp_f32 v171, v171\n\t"
    "v_exp_f32 v186, v186\n\t"
    "v_exp_f32 v187, v187\n\t"
    "v_cvt_pk_bf16_f32 v205, v170, v171\n\t"
    "ds_read_b64_tr_b16 v[138:139], v234 offset:21504\n\t"
    "ds_read_b64_tr_b16 v[144:145], v235 offset:20480\n\t"
    "ds_read_b64_tr_b16 v[146:147], v235 offset:21504\n\t"
    "v_mfma_f32_32x32x16_bf16 a[96:111], a[140:143], v[44:47], a[96:111]\n\t"
    "s_add_u32 m0, s56, 8192\n\t"
    "s_xor_b32 s80, s64, 155648\n\t"
    "buffer_load_dwordx4 v240, s[32:35], s80 offen nt lds\n\t"
    "v_add_f32 v210, v210, v170\n\t"
    "v_add_f32 v212, v212, v171\n\t"
    "v_cvt_pk_bf16_f32 v197, v186, v187\n\t"
    "v_add_f32 v211, v211, v186\n\t"
    "v_add_f32 v213, v213, v187\n\t"
    "ds_read_b64_tr_b16 v[152:153], v236 offset:20480\n\t"
    "ds_read_b64_tr_b16 v[154:155], v236 offset:21504\n\t"
    "ds_read_b64_tr_b16 v[132:133], v244 offset:22528\n\t"
    "ds_read_b64_tr_b16 v[134:135], v244 offset:23552\n\t"
    "v_mfma_f32_32x32x16_bf16 a[96:111], a[148:151], v[52:55], a[96:111]\n\t"
    "v_max3_f32 v208, v208, v172, v173\n\t"
    "v_max3_f32 v209, v209, v188, v189\n\t"
    "v_fma_f32 v172, v172, s69, v214\n\t"
    "v_fma_f32 v173, v173, s69, v214\n\t"
    "ds_read_b64_tr_b16 v[140:141], v234 offset:22528\n\t"
    "ds_read_b64_tr_b16 v[142:143], v234 offset:23552\n\t"
    "ds_read_b64_tr_b16 v[148:149], v235 offset:22528\n\t"
    "v_mfma_f32_32x32x16_bf16 a[96:111], a[156:159], v[60:63], a[96:111]\n\t"
    "buffer_load_dwordx4 v241, s[32:35], s80 offen offset:1024 nt lds\n\t"
    "v_fma_f32 v188, v188, s69, v215\n\t"
    "v_fma_f32 v189, v189, s69, v215\n\t"
    "v_exp_f32 v172, v172\n\t"
    "v_exp_f32 v173, v173\n\t"
    "v_exp_f32 v188, v188\n\t"
    "ds_read_b64_tr_b16 v[150:151], v235 offset:23552\n\t"
    "ds_read_b64_tr_b16 v[156:157], v236 offset:22528\n\t"
    "ds_read_b64_tr_b16 v[158:159], v236 offset:23552\n\t"
    "s_waitcnt lgkmcnt(14)\n\t"
    "v_mfma_f32_32x32x16_bf16 a[96:111], v[128:131], v[96:99], a[96:111]\n\t"
    "v_exp_f32 v189, v189\n\t"
    "v_cvt_pk_bf16_f32 v206, v172, v173\n\t"
    "v_add_f32 v210, v210, v172\n\t"
    "v_add_f32 v212, v212, v173\n\t"
    "v_cvt_pk_bf16_f32 v198, v188, v189\n\t"
    "s_waitcnt lgkmcnt(12)\n\t"
    "v_mfma_f32_32x32x16_bf16 a[96:111], v[136:139], a[160:163], a[96:111]\n\t"
    "buffer_load_dwordx4 v242, s[32:35], s80 offen offset:2048 nt lds\n\t"
    "v_add_f32 v211, v211, v188\n\t"
    "v_add_f32 v213, v213, v189\n\t"
    "v_max3_f32 v208, v208, v174, v175\n\t"
    "v_max3_f32 v209, v209, v190, v191\n\t"
    "v_fma_f32 v174, v174, s69, v214\n\t"
    "s_waitcnt lgkmcnt(10)\n\t"
    "v_mfma_f32_32x32x16_bf16 a[96:111], v[144:147], a[168:171], a[96:111]\n\t"
    "v_fma_f32 v175, v175, s69, v214\n\t"
    "v_fma_f32 v190, v190, s69, v215\n\t"
    "v_fma_f32 v191, v191, s69, v215\n\t"
    "v_exp_f32 v174, v174\n\t"
    "v_exp_f32 v175, v175\n\t"
    "s_waitcnt lgkmcnt(8)\n\t"
    "v_mfma_f32_32x32x16_bf16 a[96:111], v[152:155], a[176:179], a[96:111]\n\t"
    "buffer_load_dwordx4 v243, s[32:35], s80 offen offset:3072 nt lds\n\t"
    "v_exp_f32 v190, v190\n\t"
    "v_exp_f32 v191, v191\n\t"
    "v_cvt_pk_bf16_f32 v207, v174, v175\n\t"
    "v_add_f32 v210, v210, v174\n\t"
    "v_add_f32 v212, v212, v175\n\t"
    "s_waitcnt vmcnt(12)\n\t"
    "s_waitcnt lgkmcnt(0)\n\t"
    "s_barrier\n\t"
    "ds_read_b64_tr_b16 a[128:129], v244 offset:0\n\t"
    "ds_read_b64_tr_b16 a[130:131], v244 offset:1024\n\t"
    "ds_read_b64_tr_b16 a[136:137], v234 offset:0\n\t"
    "v_mfma_f32_32x32x16_bf16 a[96:111], v[132:135], v[100:103], a[96:111]\n\t"
    "s_add_u32 m0, s56, 12288\n\t"
    "s_xor_b32 s80, s64, 128\n\t"
    "buffer_load_dwordx4 v240, s[32:35], s80 offen nt lds\n\t"
    "ds_write_b128 v224, v[204:207] offset:37888\n\t"
    "v_cvt_pk_bf16_f32 v199, v190, v191\n\t"
    "v_add_f32 v211, v211, v190\n\t"
    "v_add_f32 v213, v213, v191\n\t"
    "ds_write_b128 v224, v[196:199] offset:39936\n\t"
    "ds_read_b64_tr_b16 a[138:139], v234 offset:1024\n\t"
    "ds_read_b64_tr_b16 a[144:145], v235 offset:0\n\t"
    "ds_read_b64_tr_b16 a[146:147], v235 offset:1024\n\t"
    "v_mfma_f32_32x32x16_bf16 a[96:111], v[140:143], a[164:167], a[96:111]\n\t"
    "v_mov_b32 v218, v208\n\t"
    "v_mov_b32 v219, v209\n\t"
    "s_nop 0\n\t"
    "v_permlane32_swap_b32 v218, v208\n\t"
    "v_permlane32_swap_b32 v219, v209\n\t"
    "v_max_f32 v208, v208, v218\n\t"
    "ds_read_b64_tr_b16 a[152:153], v236 offset:0\n\t"
    "ds_read_b64_tr_b16 a[154:155], v236 offset:1024\n\t"
    "ds_read_b64_tr_b16 a[132:133], v244 offset:2048\n\t"
    "ds_read_b64_tr_b16 a[134:135], v244 offset:3072\n\t"
    "v_mfma_f32_32x32x16_bf16 a[96:111], v[148:151], a[172:175], a[96:111]\n\t"
    "buffer_load_dwordx4 v241, s[32:35], s80 offen offset:1024 nt lds\n\t"
    "v_max_f32 v209, v209, v219\n\t"
    "v_add_f32 v210, v210, v212\n\t"
    "v_add_f32 v211, v211, v213\n\t"
    "v_mov_b32 v212, v210\n\t"
    "v_mov_b32 v213, v211\n\t"
    "ds_read_b64_tr_b16 a[140:141], v234 offset:2048\n\t"
    "ds_read_b64_tr_b16 a[142:143], v234 offset:3072\n\t"
    "ds_read_b64_tr_b16 a[148:149], v235 offset:2048\n\t"
    "v_mfma_f32_32x32x16_bf16 a[96:111], v[156:159], a[180:183], a[96:111]\n\t"
    "v_permlane32_swap_b32 v210, v212\n\t"
    "v_permlane32_swap_b32 v211, v213\n\t"
    "v_add_f32 v210, v210, v212\n\t"
    "v_add_f32 v211, v211, v213\n\t"
    "ds_write_b128 v224, v[208:211] offset:30720\n\t"
    "ds_read_b64_tr_b16 a[150:151], v235 offset:3072\n\t"
    "ds_read_b64_tr_b16 a[156:157], v236 offset:2048\n\t"
    "ds_read_b64_tr_b16 a[158:159], v236 offset:3072\n\t"
    "s_waitcnt lgkmcnt(14)\n\t"
    "v_mfma_f32_32x32x16_bf16 a[96:111], a[128:131], v[104:107], a[96:111]\n\t"
    "buffer_load_dwordx4 v242, s[32:35], s80 offen offset:2048 nt lds\n\t"
    "s_waitcnt lgkmcnt(13)\n\t"
    "v_mfma_f32_32x32x16_bf16 a[96:111], a[136:139], a[184:187], a[96:111]\n\t"
    "s_waitcnt lgkmcnt(11)\n\t"
    "v_mfma_f32_32x32x16_bf16 a[96:111], a[144:147], a[192:195], a[96:111]\n\t"
    "s_waitcnt lgkmcnt(9)\n\t"
    "v_mfma_f32_32x32x16_bf16 a[96:111], a[152:155], a[200:203], a[96:111]\n\t"
    "buffer_load_dwordx4 v243, s[32:35], s80 offen offset:3072 nt lds\n\t"
    "s_waitcnt vmcnt(12)\n\t"
    "s_waitcnt lgkmcnt(0)\n\t"
    "s_barrier\n\t"
    "ds_read_b64_tr_b16 v[128:129], v244 offset:4096\n\t"
    "ds_read_b64_tr_b16 v[130:131], v244 offset:5120\n\t"
    "ds_read_b64_tr_b16 v[136:137], v234 offset:4096\n\t"
    "v_mfma_f32_32x32x16_bf16 a[96:111], a[132:135], v[108:111], a[96:111]\n\t"
    "ds_read_b64_tr_b16 v[138:139], v234 offset:5120\n\t"
    "ds_read_b64_tr_b16 v[144:145], v235 offset:4096\n\t"
    "ds_read_b64_tr_b16 v[146:147], v235 offset:5120\n\t"
    "v_mfma_f32_32x32x16_bf16 a[96:111], a[140:143], a[188:191], a[96:111]\n\t"
    "ds_read_b64_tr_b16 v[152:153], v236 offset:4096\n\t"
    "ds_read_b64_tr_b16 v[154:155], v236 offset:5120\n\t"
    "ds_read_b64_tr_b16 v[132:133], v244 offset:6144\n\t"
    "ds_read_b64_tr_b16 v[134:135], v244 offset:7168\n\t"
    "v_mfma_f32_32x32x16_bf16 a[96:111], a[148:151], a[196:199], a[96:111]\n\t"
    "s_add_u32 m0, s56, 16384\n\t"
    "s_xor_b32 s80, s64, 131200\n\t"
    "buffer_load_dwordx4 v240, s[32:35], s80 offen nt lds\n\t"
    "ds_read_b64_tr_b16 v[140:141], v234 offset:6144\n\t"
    "ds_read_b64_tr_b16 v[142:143], v234 offset:7168\n\t"
    "ds_read_b64_tr_b16 v[148:149], v235 offset:6144\n\t"
    "v_mfma_f32_32x32x16_bf16 a[96:111], a[156:159], a[204:207], a[96:111]\n\t"
    "ds_read_b64_tr_b16 v[150:151], v235 offset:7168\n\t"
    "ds_read_b64_tr_b16 v[156:157], v236 offset:6144\n\t"
    "ds_read_b64_tr_b16 v[158:159], v236 offset:7168\n\t"
    "s_waitcnt lgkmcnt(14)\n\t"
    "v_mfma_f32_32x32x16_bf16 a[96:111], v[128:131], v[120:123], a[96:111]\n\t"
    "ds_read_b128 v[160:163], v251 offset:30720\n\t"
    "ds_read_b128 v[164:167], v252 offset:30720\n\t"
    "ds_read_b128 v[168:171], v253 offset:30720\n\t"
    "ds_read_b128 a[0:3], v251 offset:38912\n\t"
    "ds_read_b128 a[8:11], v252 offset:36864\n\t"
    "s_waitcnt lgkmcnt(14)\n\t"
    "v_mfma_f32_32x32x16_bf16 a[96:111], v[136:139], a[232:235], a[96:111]\n\t"
    "buffer_load_dwordx4 v241, s[32:35], s80 offen offset:1024 nt lds\n\t"
    "ds_read_b128 a[16:19], v253 offset:38912\n\t"
    "ds_read_b128 a[4:7], v251 offset:39936\n\t"
    "ds_read_b128 a[12:15], v252 offset:37888\n\t"
    "ds_read_b128 a[20:23], v253 offset:39936\n\t"
    "v_mfma_f32_32x32x16_bf16 a[96:111], v[144:147], a[240:243], a[96:111]\n\t"
    "s_waitcnt lgkmcnt(14)\n\t"
    "v_mfma_f32_32x32x16_bf16 a[96:111], v[152:155], a[248:251], a[96:111]\n\t"
    "buffer_load_dwordx4 v242, s[32:35], s80 offen offset:2048 nt lds\n\t"
    "buffer_load_dwordx4 v243, s[32:35], s80 offen offset:3072 nt lds\n\t"
    "s_waitcnt vmcnt(12)\n\t"
    "s_waitcnt lgkmcnt(0)\n\t"
    "s_barrier\n\t"
    "ds_read_b64_tr_b16 a[128:129], v244 offset:24576\n\t"
    "ds_read_b64_tr_b16 a[130:131], v244 offset:25600\n\t"
    "ds_read_b64_tr_b16 a[136:137], v234 offset:24576\n\t"
    "v_mfma_f32_32x32x16_bf16 a[96:111], v[132:135], v[124:127], a[96:111]\n\t"
    "v_max3_f32 v208, v208, v161, v164\n\t"
    "v_max3_f32 v209, v209, v160, v165\n\t"
    "v_max_f32 v208, v208, v169\n\t"
    "ds_read_b64_tr_b16 a[138:139], v234 offset:25600\n\t"
    "ds_read_b64_tr_b16 a[144:145], v235 offset:24576\n\t"
    "ds_read_b64_tr_b16 a[146:147], v235 offset:25600\n\t"
    "v_mfma_f32_32x32x16_bf16 a[96:111], v[140:143], a[236:239], a[96:111]\n\t"
    "s_add_u32 m0, s56, 20480\n\t"
    "s_xor_b32 s80, s64, 8320\n\t"
    "buffer_load_dwordx4 v240, s[32:35], s80 offen nt lds\n\t"
    "v_max_f32 v209, v209, v168\n\t"
    "v_fma_f32 v218, v208, s69, v214\n\t"
    "v_fma_f32 v219, v209, s69, v215\n\t"
    "ds_read_b64_tr_b16 a[152:153], v236 offset:24576\n\t"
    "ds_read_b64_tr_b16 a[154:155], v236 offset:25600\n\t"
    "ds_read_b64_tr_b16 a[132:133], v244 offset:26624\n\t"
    "ds_read_b64_tr_b16 a[134:135], v244 offset:27648\n\t"
    "v_mfma_f32_32x32x16_bf16 a[96:111], v[148:151], a[244:247], a[96:111]\n\t"
    "v_add_f32 v220, v210, v163\n\t"
    "v_add_f32 v221, v166, v171\n\t"
    "v_max_f32 v218, v218, v219\n\t"
    "ds_read_b64_tr_b16 a[140:141], v234 offset:26624\n\t"
    "ds_read_b64_tr_b16 a[142:143], v234 offset:27648\n\t"
    "ds_read_b64_tr_b16 a[148:149], v235 offset:26624\n\t"
    "v_mfma_f32_32x32x16_bf16 a[96:111], v[156:159], a[252:255], a[96:111]\n\t"
    "buffer_load_dwordx4 v241, s[32:35], s80 offen offset:1024 nt lds\n\t"
    "v_add_f32 v220, v220, v221\n\t"
    "v_rcp_f32 v216, v220\n\t"
    "v_cmp_lt_f32 vcc, 0x42000000, v218\n\t"
    "s_cbranch_vccnz Lredo_%=\n\t"
    "Lcont_%=:\n\t"
    "ds_read_b64_tr_b16 a[150:151], v235 offset:27648\n\t"
    "ds_read_b64_tr_b16 a[156:157], v236 offset:26624\n\t"
    "ds_read_b64_tr_b16 a[158:159], v236 offset:27648\n\t"
    "s_waitcnt lgkmcnt(14)\n\t"
    "v_mfma_f32_32x32x16_bf16 a[96:111], a[128:131], v[112:115], a[96:111]\n\t"
    "v_add_u32 v226, 40960, v250\n\t"
    "v_add_u32 v227, 81920, v250\n\t"
    "v_add_u32 v228, 122880, v250\n\t"
    "v_lshrrev_b32 v218, 4, v238\n\t"
    "s_lshl_b32 s74, s71, 3\n\t"
    "s_lshl_b32 s75, s70, 6\n\t"
    "s_waitcnt lgkmcnt(12)\n\t"
    "v_mfma_f32_32x32x16_bf16 a[96:111], a[136:139], a[208:211], a[96:111]\n\t"
    "v_add3_u32 v219, v218, s75, 63\n\t"
    "v_lshrrev_b32 v219, 6, v219\n\t"
    "v_min_u32 v219, 1, v219\n\t"
    "v_lshl_add_u32 v229, v219, 30, s74\n\t"
    "s_lshr_b32 s75, s70, 1\n\t"
    "s_or_b32 s75, s75, 0\n\t"
    "s_lshl_b32 s75, s75, 7\n\t"
    "s_waitcnt lgkmcnt(10)\n\t"
    "v_mfma_f32_32x32x16_bf16 a[96:111], a[144:147], a[216:219], a[96:111]\n\t"
    "buffer_load_dwordx4 v242, s[32:35], s80 offen offset:2048 nt lds\n\t"
    "v_lshl_add_u32 v230, v218, 3, s75\n\t"
    "v_add_u32 v219, 48, v218\n\t"
    "v_lshrrev_b32 v219, 6, v219\n\t"
    "v_lshl_add_u32 v230, v219, 30, v230\n\t"
    "v_lshrrev_b32 v219, 5, v218\n\t"
    "s_lshl_b32 s75, s70, 1\n\t"
    "s_waitcnt lgkmcnt(8)\n\t"
    "v_mfma_f32_32x32x16_bf16 a[96:111], a[152:155], a[224:227], a[96:111]\n\t"
    "v_add_u32 v219, s75, v219\n\t"
    "v_xor_b32 v220, 4, v219\n\t"
    "v_add_u32 v220, 1, v220\n\t"
    "v_lshrrev_b32 v220, 3, v220\n\t"
    "v_lshlrev_b32 v232, 30, v220\n\t"
    "s_lshr_b32 s75, s70, 1\n\t"
    "s_and_b32 s75, s75, 1\n\t"
    "buffer_load_dwordx4 v243, s[32:35], s80 offen offset:3072 nt lds\n\t"
    "s_waitcnt vmcnt(12)\n\t"
    "s_waitcnt lgkmcnt(0)\n\t"
    "s_barrier\n\t"
    "ds_read_b64_tr_b16 v[128:129], v244 offset:8192\n\t"
    "ds_read_b64_tr_b16 v[130:131], v244 offset:9216\n\t"
    "ds_read_b64_tr_b16 v[136:137], v234 offset:8192\n\t"
    "v_mfma_f32_32x32x16_bf16 a[96:111], a[132:135], v[116:119], a[96:111]\n\t"
    "v_subrev_u32 v219, s75, v219\n\t"
    "v_lshl_add_u32 v231, v219, 13, v232\n\t"
    "s_lshl_b32 s74, s71, 7\n\t"
    "v_add_u32 v231, s74, v231\n\t"
    "v_and_b32 v220, 7, v218\n\t"
    "v_lshl_add_u32 v231, v220, 4, v231\n\t"
    "v_bfe_u32 v220, v218, 3, 2\n\t"
    "ds_read_b64_tr_b16 v[138:139], v234 offset:9216\n\t"
    "ds_read_b64_tr_b16 v[144:145], v235 offset:8192\n\t"
    "ds_read_b64_tr_b16 v[146:147], v235 offset:9216\n\t"
    "v_mfma_f32_32x32x16_bf16 a[96:111], a[140:143], a[212:215], a[96:111]\n\t"
    "s_add_u32 m0, s56, 0\n\t"
    "s_xor_b32 s80, s64, 139392\n\t"
    "buffer_load_dwordx4 v240, s[32:35], s80 offen nt lds\n\t"
    "v_lshl_add_u32 v231, v220, 11, v231\n\t"
    "v_lshl_add_u32 v232, v220, 5, v232\n\t"
    "s_lshr_b32 s74, s57, 19\n\t"
    "v_add_u32 v219, s74, v219\n\t"
    "v_subrev_u32 v220, 7, v219\n\t"
    "v_min_u32 v219, v219, v220\n\t"
    "ds_read_b64_tr_b16 v[152:153], v236 offset:8192\n\t"
    "ds_read_b64_tr_b16 v[154:155], v236 offset:9216\n\t"
    "ds_read_b64_tr_b16 v[132:133], v244 offset:10240\n\t"
    "ds_read_b64_tr_b16 v[134:135], v244 offset:11264\n\t"
    "v_mfma_f32_32x32x16_bf16 a[96:111], a[148:151], a[220:223], a[96:111]\n\t"
    "v_lshl_add_u32 v232, v219, 7, v232\n\t"
    "v_bfe_u32 v220, v218, 1, 1\n\t"
    "v_lshl_add_u32 v232, v220, 4, v232\n\t"
    "s_lshr_b32 s74, s71, 3\n\t"
    "s_lshl_b32 s74, s74, 5\n\t"
    "s_and_b32 s75, s71, 7\n\t"
    "s_lshl_b32 s75, s75, 1\n\t"
    "ds_read_b64_tr_b16 v[140:141], v234 offset:10240\n\t"
    "ds_read_b64_tr_b16 v[142:143], v234 offset:11264\n\t"
    "ds_read_b64_tr_b16 v[148:149], v235 offset:10240\n\t"
    "v_mfma_f32_32x32x16_bf16 a[96:111], a[156:159], a[228:231], a[96:111]\n\t"
    "buffer_load_dwordx4 v241, s[32:35], s80 offen offset:1024 nt lds\n\t"
    "s_add_u32 s74, s74, s75\n\t"
    "v_bfe_u32 v220, v218, 2, 1\n\t"
    "v_and_b32 v219, 1, v218\n\t"
    "v_lshl_add_u32 v220, v219, 4, v220\n\t"
    "v_add_u32 v220, s74, v220\n\t"
    "v_mul_u32_u24 v220, 1792, v220\n\t"
    "v_add_u32 v232, v220, v232\n\t"
    "ds_read_b64_tr_b16 v[150:151], v235 offset:11264\n\t"
    "ds_read_b64_tr_b16 v[156:157], v236 offset:10240\n\t"
    "ds_read_b64_tr_b16 v[158:159], v236 offset:11264\n\t"
    "s_waitcnt lgkmcnt(14)\n\t"
    "v_mfma_f32_32x32x16_bf16 a[96:111], v[128:131], v[200:203], a[96:111]\n\t"
    "buffer_load_dwordx4 v242, s[32:35], s80 offen offset:2048 nt lds\n\t"
    "s_waitcnt lgkmcnt(12)\n\t"
    "v_mfma_f32_32x32x16_bf16 a[96:111], v[136:139], a[0:3], a[96:111]\n\t"
    "s_waitcnt lgkmcnt(10)\n\t"
    "v_mfma_f32_32x32x16_bf16 a[96:111], v[144:147], a[8:11], a[96:111]\n\t"
    "s_waitcnt lgkmcnt(8)\n\t"
    "v_mfma_f32_32x32x16_bf16 a[96:111], v[152:155], a[16:19], a[96:111]\n\t"
    "s_waitcnt vmcnt(11)\n\t"
    "s_waitcnt lgkmcnt(0)\n\t"
    "s_barrier\n\t"
    "ds_read_b64_tr_b16 a[128:129], v244 offset:12288\n\t"
    "ds_read_b64_tr_b16 a[130:131], v244 offset:13312\n\t"
    "ds_read_b64_tr_b16 a[136:137], v234 offset:12288\n\t"
    "v_mfma_f32_32x32x16_bf16 a[96:111], v[132:135], v[204:207], a[96:111]\n\t"
    "buffer_load_dwordx4 v243, s[32:35], s80 offen offset:3072 nt lds\n\t"
    "ds_read_b64_tr_b16 a[138:139], v234 offset:13312\n\t"
    "ds_read_b64_tr_b16 a[144:145], v235 offset:12288\n\t"
    "ds_read_b64_tr_b16 a[146:147], v235 offset:13312\n\t"
    "v_mfma_f32_32x32x16_bf16 a[96:111], v[140:143], a[4:7], a[96:111]\n\t"
    "ds_read_b64_tr_b16 a[152:153], v236 offset:12288\n\t"
    "ds_read_b64_tr_b16 a[154:155], v236 offset:13312\n\t"
    "ds_read_b64_tr_b16 a[132:133], v244 offset:14336\n\t"
    "ds_read_b64_tr_b16 a[134:135], v244 offset:15360\n\t"
    "v_mfma_f32_32x32x16_bf16 a[96:111], v[148:151], a[12:15], a[96:111]\n\t"
    "s_add_u32 m0, s56, 4096\n\t"
    "s_xor_b32 s80, s64, 16512\n\t"
    "buffer_load_dwordx4 v240, s[32:35], s80 offen nt lds\n\t"
    "ds_read_b64_tr_b16 a[140:141], v234 offset:14336\n\t"
    "ds_read_b64_tr_b16 a[142:143], v234 offset:15360\n\t"
    "ds_read_b64_tr_b16 a[148:149], v235 offset:14336\n\t"
    "v_mfma_f32_32x32x16_bf16 a[96:111], v[156:159], a[20:23], a[96:111]\n\t"
    "ds_read_b64_tr_b16 a[150:151], v235 offset:15360\n\t"
    "ds_read_b64_tr_b16 a[156:157], v236 offset:14336\n\t"
    "ds_read_b64_tr_b16 a[158:159], v236 offset:15360\n\t"
    "s_waitcnt lgkmcnt(14)\n\t"
    "v_mfma_f32_32x32x16_bf16 v[160:175], a[128:131], v[0:3], 0\n\t"
    "buffer_load_dwordx4 v241, s[32:35], s80 offen offset:1024 nt lds\n\t"
    "s_waitcnt lgkmcnt(12)\n\t"
    "v_mfma_f32_32x32x16_bf16 v[160:175], a[136:139], v[8:11], v[160:175]\n\t"
    "s_waitcnt lgkmcnt(10)\n\t"
    "v_mfma_f32_32x32x16_bf16 v[160:175], a[144:147], v[16:19], v[160:175]\n\t"
    "s_nop 1\n\t"
    "v_accvgpr_read_b32 v176, a96\n\t"
    "v_accvgpr_read_b32 v177, a97\n\t"
    "v_accvgpr_read_b32 v178, a98\n\t"
    "v_accvgpr_read_b32 v179, a99\n\t"
    "v_accvgpr_read_b32 v180, a100\n\t"
    "v_accvgpr_read_b32 v181, a101\n\t"
    "v_accvgpr_read_b32 v182, a102\n\t"
    "s_waitcnt lgkmcnt(8)\n\t"
    "v_mfma_f32_32x32x16_bf16 v[160:175], a[152:155], v[24:27], v[160:175]\n\t"
    "buffer_load_dwordx4 v242, s[32:35], s80 offen offset:2048 nt lds\n\t"
    "v_accvgpr_read_b32 v183, a103\n\t"
    "v_accvgpr_read_b32 v184, a104\n\t"
    "v_accvgpr_read_b32 v185, a105\n\t"
    "v_accvgpr_read_b32 v186, a106\n\t"
    "v_accvgpr_read_b32 v187, a107\n\t"
    "v_accvgpr_read_b32 v188, a108\n\t"
    "v_accvgpr_read_b32 v189, a109\n\t"
    "buffer_load_dwordx4 v243, s[32:35], s80 offen offset:3072 nt lds\n\t"
    "s_waitcnt vmcnt(12)\n\t"
    "s_waitcnt lgkmcnt(0)\n\t"
    "s_barrier\n\t"
    "ds_read_b64_tr_b16 v[128:129], v244 offset:16384\n\t"
    "ds_read_b64_tr_b16 v[130:131], v244 offset:17408\n\t"
    "ds_read_b64_tr_b16 v[136:137], v234 offset:16384\n\t"
    "v_mfma_f32_32x32x16_bf16 v[160:175], a[132:135], v[4:7], v[160:175]\n\t"
    "v_accvgpr_read_b32 v190, a110\n\t"
    "v_accvgpr_read_b32 v191, a111\n\t"
    "v_mul_f32 v176, v176, v216\n\t"
    "v_mul_f32 v177, v177, v216\n\t"
    "v_mul_f32 v178, v178, v216\n\t"
    "v_mul_f32 v179, v179, v216\n\t"
    "v_mul_f32 v184, v184, v216\n\t"
    "ds_read_b64_tr_b16 v[138:139], v234 offset:17408\n\t"
    "ds_read_b64_tr_b16 v[144:145], v235 offset:16384\n\t"
    "ds_read_b64_tr_b16 v[146:147], v235 offset:17408\n\t"
    "v_mfma_f32_32x32x16_bf16 v[160:175], a[140:143], v[12:15], v[160:175]\n\t"
    "s_add_u32 m0, s56, 24576\n\t"
    "s_xor_b32 s80, s64, 147584\n\t"
    "buffer_load_dwordx4 v240, s[32:35], s80 offen nt lds\n\t"
    "v_mul_f32 v185, v185, v216\n\t"
    "v_mul_f32 v186, v186, v216\n\t"
    "v_mul_f32 v187, v187, v216\n\t"
    "v_cvt_pk_bf16_f32 v176, v176, v177\n\t"
    "v_cvt_pk_bf16_f32 v177, v178, v179\n\t"
    "v_cvt_pk_bf16_f32 v178, v184, v185\n\t"
    "v_cvt_pk_bf16_f32 v179, v186, v187\n\t"
    "ds_read_b64_tr_b16 v[152:153], v236 offset:16384\n\t"
    "ds_read_b64_tr_b16 v[154:155], v236 offset:17408\n\t"
    "ds_read_b64_tr_b16 v[132:133], v244 offset:18432\n\t"
    "ds_read_b64_tr_b16 v[134:135], v244 offset:19456\n\t"
    "v_mfma_f32_32x32x16_bf16 v[160:175], a[148:151], v[20:23], v[160:175]\n\t"
    "ds_write_b128 v237, v[176:179] offset:28672\n\t"
    "v_mul_f32 v180, v180, v216\n\t"
    "v_mul_f32 v181, v181, v216\n\t"
    "v_mul_f32 v182, v182, v216\n\t"
    "v_mul_f32 v183, v183, v216\n\t"
    "v_mul_f32 v188, v188, v216\n\t"
    "v_mul_f32 v189, v189, v216\n\t"
    "ds_read_b64_tr_b16 v[140:141], v234 offset:18432\n\t"
    "ds_read_b64_tr_b16 v[142:143], v234 offset:19456\n\t"
    "ds_read_b64_tr_b16 v[148:149], v235 offset:18432\n\t"
    "v_mfma_f32_32x32x16_bf16 v[160:175], a[156:159], v[28:31], v[160:175]\n\t"
    "buffer_load_dwordx4 v241, s[32:35], s80 offen offset:1024 nt lds\n\t"
    "v_mul_f32 v190, v190, v216\n\t"
    "v_mul_f32 v191, v191, v216\n\t"
    "v_cvt_pk_bf16_f32 v180, v180, v181\n\t"
    "v_cvt_pk_bf16_f32 v181, v182, v183\n\t"
    "v_cvt_pk_bf16_f32 v182, v188, v189\n\t"
    "v_cvt_pk_bf16_f32 v183, v190, v191\n\t"
    "ds_write_b128 v237, v[180:183] offset:29184\n\t"
    "ds_read_b64_tr_b16 v[150:151], v235 offset:19456\n\t"
    "ds_read_b64_tr_b16 v[156:157], v236 offset:18432\n\t"
    "ds_read_b64_tr_b16 v[158:159], v236 offset:19456\n\t"
    "s_waitcnt lgkmcnt(14)\n\t"
    "v_mfma_f32_32x32x16_bf16 v[160:175], v[128:131], v[64:67], v[160:175]\n\t"
    "buffer_load_dwordx4 v242, s[32:35], s80 offen offset:2048 nt lds\n\t"
    "v_mfma_f32_32x32x16_bf16 v[160:175], v[136:139], v[72:75], v[160:175]\n\t"
    "s_waitcnt lgkmcnt(12)\n\t"
    "v_mfma_f32_32x32x16_bf16 v[160:175], v[144:147], v[80:83], v[160:175]\n\t"
    "s_waitcnt lgkmcnt(10)\n\t"
    "v_mfma_f32_32x32x16_bf16 v[160:175], v[152:155], v[88:91], v[160:175]\n\t"
    "buffer_load_dwordx4 v243, s[32:35], s80 offen offset:3072 nt lds\n\t"
    "s_waitcnt vmcnt(12)\n\t"
    "s_waitcnt lgkmcnt(0)\n\t"
    "s_barrier\n\t"
    "ds_read_b64_tr_b16 a[128:129], v244 offset:20480\n\t"
    "ds_read_b64_tr_b16 a[130:131], v244 offset:21504\n\t"
    "ds_read_b64_tr_b16 a[136:137], v234 offset:20480\n\t"
    "v_mfma_f32_32x32x16_bf16 v[160:175], v[132:135], v[68:71], v[160:175]\n\t"
    "ds_read_b64_tr_b16 a[138:139], v234 offset:21504\n\t"
    "ds_read_b64_tr_b16 a[144:145], v235 offset:20480\n\t"
    "ds_read_b64_tr_b16 a[146:147], v235 offset:21504\n\t"
    "v_mfma_f32_32x32x16_bf16 v[160:175], v[140:143], v[76:79], v[160:175]\n\t"
    "s_add_u32 m0, s56, 32768\n\t"
    "s_xor_b32 s80, s64, 24704\n\t"
    "buffer_load_dwordx4 v240, s[32:35], s80 offen nt lds\n\t"
    "ds_read_b64_tr_b16 a[152:153], v236 offset:20480\n\t"
    "ds_read_b64_tr_b16 a[154:155], v236 offset:21504\n\t"
    "ds_read_b64_tr_b16 a[132:133], v244 offset:22528\n\t"
    "ds_read_b64_tr_b16 a[134:135], v244 offset:23552\n\t"
    "v_mfma_f32_32x32x16_bf16 v[160:175], v[148:151], v[84:87], v[160:175]\n\t"
    "ds_read_b64_tr_b16 a[140:141], v234 offset:22528\n\t"
    "ds_read_b64_tr_b16 a[142:143], v234 offset:23552\n\t"
    "ds_read_b64_tr_b16 a[148:149], v235 offset:22528\n\t"
    "v_mfma_f32_32x32x16_bf16 v[160:175], v[156:159], v[92:95], v[160:175]\n\t"
    "ds_read_b64_tr_b16 a[150:151], v235 offset:23552\n\t"
    "ds_read_b64_tr_b16 a[156:157], v236 offset:22528\n\t"
    "ds_read_b64_tr_b16 a[158:159], v236 offset:23552\n\t"
    "s_waitcnt lgkmcnt(14)\n\t"
    "v_mfma_f32_32x32x16_bf16 v[160:175], a[128:131], v[32:35], v[160:175]\n\t"
    "buffer_load_dwordx4 v241, s[32:35], s80 offen offset:1024 nt lds\n\t"
    "ds_read_b128 a[32:35], v254 offset:0\n\t"
    "ds_read_b128 a[36:39], v254 offset:1024\n\t"
    "ds_read_b128 a[40:43], v254 offset:40960\n\t"
    "ds_read_b128 a[44:47], v254 offset:41984\n\t"
    "s_waitcnt lgkmcnt(14)\n\t"
    "v_mfma_f32_32x32x16_bf16 v[160:175], a[136:139], v[40:43], v[160:175]\n\t"
    "ds_read_b128 a[48:51], v255 offset:0\n\t"
    "ds_read_b128 a[52:55], v255 offset:1024\n\t"
    "ds_read_b128 a[56:59], v255 offset:40960\n\t"
    "ds_read_b128 a[60:63], v255 offset:41984\n\t"
    "v_mfma_f32_32x32x16_bf16 v[160:175], a[144:147], v[48:51], v[160:175]\n\t"
    "s_waitcnt lgkmcnt(14)\n\t"
    "v_mfma_f32_32x32x16_bf16 v[160:175], a[152:155], v[56:59], v[160:175]\n\t"
    "buffer_load_dwordx4 v242, s[32:35], s80 offen offset:2048 nt lds\n\t"
    "buffer_load_dwordx4 v243, s[32:35], s80 offen offset:3072 nt lds\n\t"
    "s_waitcnt vmcnt(12)\n\t"
    "s_waitcnt lgkmcnt(0)\n\t"
    "s_barrier\n\t"
    "ds_read_b64_tr_b16 v[128:129], v244 offset:0\n\t"
    "ds_read_b64_tr_b16 v[130:131], v244 offset:1024\n\t"
    "ds_read_b64_tr_b16 v[136:137], v234 offset:0\n\t"
    "v_mfma_f32_32x32x16_bf16 v[160:175], a[132:135], v[36:39], v[160:175]\n\t"
    "ds_read_b64_tr_b16 v[138:139], v234 offset:1024\n\t"
    "ds_read_b64_tr_b16 v[144:145], v235 offset:0\n\t"
    "ds_read_b64_tr_b16 v[146:147], v235 offset:1024\n\t"
    "v_mfma_f32_32x32x16_bf16 v[160:175], a[140:143], v[44:47], v[160:175]\n\t"
    "ds_read_b64_tr_b16 v[152:153], v236 offset:0\n\t"
    "ds_read_b64_tr_b16 v[154:155], v236 offset:1024\n\t"
    "ds_read_b64_tr_b16 v[132:133], v244 offset:2048\n\t"
    "ds_read_b64_tr_b16 v[134:135], v244 offset:3072\n\t"
    "v_mfma_f32_32x32x16_bf16 v[160:175], a[148:151], v[52:55], v[160:175]\n\t"
    "s_add_u32 m0, s56, 36864\n\t"
    "s_xor_b32 s80, s64, 155776\n\t"
    "buffer_load_dwordx4 v240, s[32:35], s80 offen nt lds\n\t"
    "ds_read_b64_tr_b16 v[140:141], v234 offset:2048\n\t"
    "ds_read_b64_tr_b16 v[142:143], v234 offset:3072\n\t"
    "ds_read_b64_tr_b16 v[148:149], v235 offset:2048\n\t"
    "v_mfma_f32_32x32x16_bf16 v[160:175], a[156:159], v[60:63], v[160:175]\n\t"
    "ds_read_b64_tr_b16 v[150:151], v235 offset:3072\n\t"
    "ds_read_b64_tr_b16 v[156:157], v236 offset:2048\n\t"
    "ds_read_b64_tr_b16 v[158:159], v236 offset:3072\n\t"
    "s_waitcnt lgkmcnt(14)\n\t"
    "v_mfma_f32_32x32x16_bf16 v[160:175], v[128:131], v[96:99], v[160:175]\n\t"
    "s_waitcnt lgkmcnt(12)\n\t"
    "v_mfma_f32_32x32x16_bf16 v[160:175], v[136:139], a[160:163], v[160:175]\n\t"
    "buffer_load_dwordx4 v241, s[32:35], s80 offen offset:1024 nt lds\n\t"
    "s_waitcnt lgkmcnt(10)\n\t"
    "v_mfma_f32_32x32x16_bf16 v[160:175], v[144:147], a[168:171], v[160:175]\n\t"
    "s_waitcnt lgkmcnt(8)\n\t"
    "v_mfma_f32_32x32x16_bf16 v[160:175], v[152:155], a[176:179], v[160:175]\n\t"
    "buffer_load_dwordx4 v242, s[32:35], s80 offen offset:2048 nt lds\n\t"
    "buffer_load_dwordx4 v243, s[32:35], s80 offen offset:3072 nt lds\n\t"
    "s_waitcnt vmcnt(12)\n\t"
    "s_waitcnt lgkmcnt(0)\n\t"
    "s_barrier\n\t"
    "ds_read_b64_tr_b16 a[128:129], v244 offset:4096\n\t"
    "ds_read_b64_tr_b16 a[130:131], v244 offset:5120\n\t"
    "ds_read_b64_tr_b16 a[136:137], v234 offset:4096\n\t"
    "v_mfma_f32_32x32x16_bf16 v[160:175], v[132:135], v[100:103], v[160:175]\n\t"
    "ds_read_b64_tr_b16 a[138:139], v234 offset:5120\n\t"
    "ds_read_b64_tr_b16 a[144:145], v235 offset:4096\n\t"
    "ds_read_b64_tr_b16 a[146:147], v235 offset:5120\n\t"
    "v_mfma_f32_32x32x16_bf16 v[160:175], v[140:143], a[164:167], v[160:175]\n\t"
    "ds_read_b64_tr_b16 a[152:153], v236 offset:4096\n\t"
    "ds_read_b64_tr_b16 a[154:155], v236 offset:5120\n\t"
    "ds_read_b64_tr_b16 a[132:133], v244 offset:6144\n\t"
    "ds_read_b64_tr_b16 a[134:135], v244 offset:7168\n\t"
    "v_mfma_f32_32x32x16_bf16 v[160:175], v[148:151], a[172:175], v[160:175]\n\t"
    "s_add_u32 m0, s56, 8192\n\t"
    "s_xor_b32 s80, s64, 256\n\t"
    "buffer_load_dwordx4 v240, s[32:35], s80 offen nt lds\n\t"
    "ds_read_b64_tr_b16 a[140:141], v234 offset:6144\n\t"
    "ds_read_b64_tr_b16 a[142:143], v234 offset:7168\n\t"
    "ds_read_b64_tr_b16 a[148:149], v235 offset:6144\n\t"
    "v_mfma_f32_32x32x16_bf16 v[160:175], v[156:159], a[180:183], v[160:175]\n\t"
    "ds_read_b64_tr_b16 a[150:151], v235 offset:7168\n\t"
    "ds_read_b64_tr_b16 a[156:157], v236 offset:6144\n\t"
    "ds_read_b64_tr_b16 a[158:159], v236 offset:7168\n\t"
    "s_waitcnt lgkmcnt(14)\n\t"
    "v_mfma_f32_32x32x16_bf16 v[160:175], a[128:131], v[104:107], v[160:175]\n\t"
    "s_waitcnt lgkmcnt(12)\n\t"
    "v_mfma_f32_32x32x16_bf16 v[160:175], a[136:139], a[184:187], v[160:175]\n\t"
    "buffer_load_dwordx4 v241, s[32:35], s80 offen offset:1024 nt lds\n\t"
    "s_waitcnt lgkmcnt(10)\n\t"
    "v_mfma_f32_32x32x16_bf16 v[160:175], a[144:147], a[192:195], v[160:175]\n\t"
    "s_waitcnt lgkmcnt(8)\n\t"
    "v_mfma_f32_32x32x16_bf16 v[160:175], a[152:155], a[200:203], v[160:175]\n\t"
    "buffer_load_dwordx4 v242, s[32:35], s80 offen offset:2048 nt lds\n\t"
    "buffer_load_dwordx4 v243, s[32:35], s80 offen offset:3072 nt lds\n\t"
    "s_waitcnt vmcnt(12)\n\t"
    "s_waitcnt lgkmcnt(0)\n\t"
    "s_barrier\n\t"
    "ds_read_b64_tr_b16 v[128:129], v244 offset:24576\n\t"
    "ds_read_b64_tr_b16 v[130:131], v244 offset:25600\n\t"
    "ds_read_b64_tr_b16 v[136:137], v234 offset:24576\n\t"
    "v_mfma_f32_32x32x16_bf16 v[160:175], a[132:135], v[108:111], v[160:175]\n\t"
    "ds_read_b64_tr_b16 v[138:139], v234 offset:25600\n\t"
    "ds_read_b64_tr_b16 v[144:145], v235 offset:24576\n\t"
    "ds_read_b64_tr_b16 v[146:147], v235 offset:25600\n\t"
    "v_mfma_f32_32x32x16_bf16 v[160:175], a[140:143], a[188:191], v[160:175]\n\t"
    "ds_read_b64_tr_b16 v[152:153], v236 offset:24576\n\t"
    "ds_read_b64_tr_b16 v[154:155], v236 offset:25600\n\t"
    "ds_read_b64_tr_b16 v[132:133], v244 offset:26624\n\t"
    "ds_read_b64_tr_b16 v[134:135], v244 offset:27648\n\t"
    "v_mfma_f32_32x32x16_bf16 v[160:175], a[148:151], a[196:199], v[160:175]\n\t"
    "s_add_u32 m0, s56, 12288\n\t"
    "s_xor_b32 s80, s64, 131328\n\t"
    "buffer_load_dwordx4 v240, s[32:35], s80 offen nt lds\n\t"
    "ds_read_b64_tr_b16 v[140:141], v234 offset:26624\n\t"
    "ds_read_b64_tr_b16 v[142:143], v234 offset:27648\n\t"
    "ds_read_b64_tr_b16 v[148:149], v235 offset:26624\n\t"
    "v_mfma_f32_32x32x16_bf16 v[160:175], a[156:159], a[204:207], v[160:175]\n\t"
    "ds_read_b64_tr_b16 v[150:151], v235 offset:27648\n\t"
    "ds_read_b64_tr_b16 v[156:157], v236 offset:26624\n\t"
    "ds_read_b64_tr_b16 v[158:159], v236 offset:27648\n\t"
    "s_waitcnt lgkmcnt(14)\n\t"
    "v_mfma_f32_32x32x16_bf16 v[160:175], v[128:131], v[120:123], v[160:175]\n\t"
    "s_waitcnt lgkmcnt(12)\n\t"
    "v_mfma_f32_32x32x16_bf16 v[160:175], v[136:139], a[232:235], v[160:175]\n\t"
    "buffer_load_dwordx4 v241, s[32:35], s80 offen offset:1024 nt lds\n\t"
    "s_waitcnt lgkmcnt(10)\n\t"
    "v_mfma_f32_32x32x16_bf16 v[160:175], v[144:147], a[240:243], v[160:175]\n\t"
    "s_waitcnt lgkmcnt(8)\n\t"
    "v_mfma_f32_32x32x16_bf16 v[160:175], v[152:155], a[248:251], v[160:175]\n\t"
    "buffer_load_dwordx4 v242, s[32:35], s80 offen offset:2048 nt lds\n\t"
    "buffer_load_dwordx4 v243, s[32:35], s80 offen offset:3072 nt lds\n\t"
    "s_waitcnt vmcnt(12)\n\t"
    "s_waitcnt lgkmcnt(0)\n\t"
    "s_barrier\n\t"
    "ds_read_b64_tr_b16 a[128:129], v244 offset:32768\n\t"
    "ds_read_b64_tr_b16 a[130:131], v244 offset:33792\n\t"
    "ds_read_b64_tr_b16 a[136:137], v234 offset:32768\n\t"
    "v_mfma_f32_32x32x16_bf16 v[160:175], v[132:135], v[124:127], v[160:175]\n\t"
    "ds_read_b64_tr_b16 a[138:139], v234 offset:33792\n\t"
    "ds_read_b64_tr_b16 a[144:145], v235 offset:32768\n\t"
    "ds_read_b64_tr_b16 a[146:147], v235 offset:33792\n\t"
    "v_mfma_f32_32x32x16_bf16 v[160:175], v[140:143], a[236:239], v[160:175]\n\t"
    "ds_read_b64_tr_b16 a[152:153], v236 offset:32768\n\t"
    "ds_read_b64_tr_b16 a[154:155], v236 offset:33792\n\t"
    "ds_read_b64_tr_b16 a[132:133], v244 offset:34816\n\t"
    "ds_read_b64_tr_b16 a[134:135], v244 offset:35840\n\t"
    "v_mfma_f32_32x32x16_bf16 v[160:175], v[148:151], a[244:247], v[160:175]\n\t"
    "s_add_u32 m0, s56, 16384\n\t"
    "s_xor_b32 s80, s64, 8448\n\t"
    "buffer_load_dwordx4 v240, s[32:35], s80 offen nt lds\n\t"
    "ds_read_b64_tr_b16 a[140:141], v234 offset:34816\n\t"
    "ds_read_b64_tr_b16 a[142:143], v234 offset:35840\n\t"
    "ds_read_b64_tr_b16 a[148:149], v235 offset:34816\n\t"
    "v_mfma_f32_32x32x16_bf16 v[160:175], v[156:159], a[252:255], v[160:175]\n\t"
    "ds_read_b64_tr_b16 a[150:151], v235 offset:35840\n\t"
    "ds_read_b64_tr_b16 a[156:157], v236 offset:34816\n\t"
    "ds_read_b64_tr_b16 a[158:159], v236 offset:35840\n\t"
    "s_waitcnt lgkmcnt(14)\n\t"
    "v_mfma_f32_32x32x16_bf16 v[160:175], a[128:131], v[112:115], v[160:175]\n\t"
    "s_waitcnt lgkmcnt(12)\n\t"
    "v_mfma_f32_32x32x16_bf16 v[160:175], a[136:139], a[208:211], v[160:175]\n\t"
    "buffer_load_dwordx4 v241, s[32:35], s80 offen offset:1024 nt lds\n\t"
    "s_waitcnt lgkmcnt(10)\n\t"
    "v_mfma_f32_32x32x16_bf16 v[160:175], a[144:147], a[216:219], v[160:175]\n\t"
    "s_waitcnt lgkmcnt(8)\n\t"
    "v_mfma_f32_32x32x16_bf16 v[160:175], a[152:155], a[224:227], v[160:175]\n\t"
    "buffer_load_dwordx4 v242, s[32:35], s80 offen offset:2048 nt lds\n\t"
    "buffer_load_dwordx4 v243, s[32:35], s80 offen offset:3072 nt lds\n\t"
    "s_waitcnt vmcnt(12)\n\t"
    "s_waitcnt lgkmcnt(0)\n\t"
    "s_barrier\n\t"
    "ds_read_b64_tr_b16 v[128:129], v244 offset:36864\n\t"
    "ds_read_b64_tr_b16 v[130:131], v244 offset:37888\n\t"
    "ds_read_b64_tr_b16 v[136:137], v234 offset:36864\n\t"
    "v_mfma_f32_32x32x16_bf16 v[160:175], a[132:135], v[116:119], v[160:175]\n\t"
    "ds_read_b64_tr_b16 v[138:139], v234 offset:37888\n\t"
    "ds_read_b64_tr_b16 v[144:145], v235 offset:36864\n\t"
    "ds_read_b64_tr_b16 v[146:147], v235 offset:37888\n\t"
    "v_mfma_f32_32x32x16_bf16 v[160:175], a[140:143], a[212:215], v[160:175]\n\t"
    "ds_read_b64_tr_b16 v[152:153], v236 offset:36864\n\t"
    "ds_read_b64_tr_b16 v[154:155], v236 offset:37888\n\t"
    "ds_read_b64_tr_b16 v[132:133], v244 offset:38912\n\t"
    "ds_read_b64_tr_b16 v[134:135], v244 offset:39936\n\t"
    "v_mfma_f32_32x32x16_bf16 v[160:175], a[148:151], a[220:223], v[160:175]\n\t"
    "s_add_u32 m0, s56, 20480\n\t"
    "s_xor_b32 s80, s64, 139520\n\t"
    "buffer_load_dwordx4 v240, s[32:35], s80 offen nt lds\n\t"
    "ds_read_b64_tr_b16 v[140:141], v234 offset:38912\n\t"
    "ds_read_b64_tr_b16 v[142:143], v234 offset:39936\n\t"
    "ds_read_b64_tr_b16 v[148:149], v235 offset:38912\n\t"
    "v_mfma_f32_32x32x16_bf16 v[160:175], a[156:159], a[228:231], v[160:175]\n\t"
    "ds_read_b64_tr_b16 v[150:151], v235 offset:39936\n\t"
    "ds_read_b64_tr_b16 v[156:157], v236 offset:38912\n\t"
    "ds_read_b64_tr_b16 v[158:159], v236 offset:39936\n\t"
    "s_waitcnt lgkmcnt(14)\n\t"
    "v_mfma_f32_32x32x16_bf16 v[160:175], v[128:131], v[200:203], v[160:175]\n\t"
    "s_waitcnt lgkmcnt(12)\n\t"
    "v_mfma_f32_32x32x16_bf16 v[160:175], v[136:139], a[0:3], v[160:175]\n\t"
    "buffer_load_dwordx4 v241, s[32:35], s80 offen offset:1024 nt lds\n\t"
    "s_waitcnt lgkmcnt(10)\n\t"
    "v_mfma_f32_32x32x16_bf16 v[160:175], v[144:147], a[8:11], v[160:175]\n\t"
    "s_waitcnt lgkmcnt(8)\n\t"
    "v_mfma_f32_32x32x16_bf16 v[160:175], v[152:155], a[16:19], v[160:175]\n\t"
    "buffer_load_dwordx4 v242, s[32:35], s80 offen offset:2048 nt lds\n\t"
    "buffer_load_dwordx4 v243, s[32:35], s80 offen offset:3072 nt lds\n\t"
    "s_waitcnt vmcnt(12)\n\t"
    "s_waitcnt lgkmcnt(0)\n\t"
    "s_barrier\n\t"
    "ds_read_b64_tr_b16 a[128:129], v244 offset:8192\n\t"
    "ds_read_b64_tr_b16 a[130:131], v244 offset:9216\n\t"
    "ds_read_b64_tr_b16 a[136:137], v234 offset:8192\n\t"
    "v_mfma_f32_32x32x16_bf16 v[160:175], v[132:135], v[204:207], v[160:175]\n\t"
    "ds_read_b64_tr_b16 a[138:139], v234 offset:9216\n\t"
    "ds_read_b64_tr_b16 a[144:145], v235 offset:8192\n\t"
    "ds_read_b64_tr_b16 a[146:147], v235 offset:9216\n\t"
    "v_mfma_f32_32x32x16_bf16 v[160:175], v[140:143], a[4:7], v[160:175]\n\t"
    "ds_read_b64_tr_b16 a[152:153], v236 offset:8192\n\t"
    "ds_read_b64_tr_b16 a[154:155], v236 offset:9216\n\t"
    "ds_read_b64_tr_b16 a[132:133], v244 offset:10240\n\t"
    "ds_read_b64_tr_b16 a[134:135], v244 offset:11264\n\t"
    "v_mfma_f32_32x32x16_bf16 v[160:175], v[148:151], a[12:15], v[160:175]\n\t"
    "s_add_u32 m0, s56, 0\n\t"
    "s_xor_b32 s80, s64, 16640\n\t"
    "buffer_load_dwordx4 v240, s[32:35], s80 offen nt lds\n\t"
    "ds_read_b64_tr_b16 a[140:141], v234 offset:10240\n\t"
    "ds_read_b64_tr_b16 a[142:143], v234 offset:11264\n\t"
    "ds_read_b64_tr_b16 a[148:149], v235 offset:10240\n\t"
    "v_mfma_f32_32x32x16_bf16 v[160:175], v[156:159], a[20:23], v[160:175]\n\t"
    "ds_read_b64_tr_b16 a[150:151], v235 offset:11264\n\t"
    "ds_read_b64_tr_b16 a[156:157], v236 offset:10240\n\t"
    "ds_read_b64_tr_b16 a[158:159], v236 offset:11264\n\t"
    "s_waitcnt lgkmcnt(14)\n\t"
    "v_mfma_f32_32x32x16_bf16 v[176:191], a[128:131], v[0:3], 0\n\t"
    "s_waitcnt lgkmcnt(12)\n\t"
    "v_mfma_f32_32x32x16_bf16 v[176:191], a[136:139], v[8:11], v[176:191]\n\t"
    "buffer_load_dwordx4 v241, s[32:35], s80 offen offset:1024 nt lds\n\t"
    "s_waitcnt lgkmcnt(10)\n\t"
    "v_mfma_f32_32x32x16_bf16 v[176:191], a[144:147], v[16:19], v[176:191]\n\t"
    "s_nop 1\n\t"
    "v_mul_f32 v160, v160, v216\n\t"
    "v_mul_f32 v161, v161, v216\n\t"
    "v_mul_f32 v162, v162, v216\n\t"
    "v_mul_f32 v163, v163, v216\n\t"
    "s_waitcnt lgkmcnt(8)\n\t"
    "v_mfma_f32_32x32x16_bf16 v[176:191], a[152:155], v[24:27], v[176:191]\n\t"
    "v_mul_f32 v168, v168, v216\n\t"
    "v_mul_f32 v169, v169, v216\n\t"
    "v_mul_f32 v170, v170, v216\n\t"
    "v_mul_f32 v171, v171, v216\n\t"
    "buffer_load_dwordx4 v242, s[32:35], s80 offen offset:2048 nt lds\n\t"
    "buffer_load_dwordx4 v243, s[32:35], s80 offen offset:3072 nt lds\n\t"
    "s_waitcnt vmcnt(12)\n\t"
    "s_waitcnt lgkmcnt(0)\n\t"
    "s_barrier\n\t"
    "ds_read_b64_tr_b16 v[128:129], v244 offset:12288\n\t"
    "ds_read_b64_tr_b16 v[130:131], v244 offset:13312\n\t"
    "ds_read_b64_tr_b16 v[136:137], v234 offset:12288\n\t"
    "v_mfma_f32_32x32x16_bf16 v[176:191], a[132:135], v[4:7], v[176:191]\n\t"
    "v_cvt_pk_bf16_f32 v160, v160, v161\n\t"
    "v_cvt_pk_bf16_f32 v161, v162, v163\n\t"
    "v_cvt_pk_bf16_f32 v162, v168, v169\n\t"
    "v_cvt_pk_bf16_f32 v163, v170, v171\n\t"
    "ds_write_b128 v237, v[160:163] offset:30720\n\t"
    "ds_read_b64_tr_b16 v[138:139], v234 offset:13312\n\t"
    "ds_read_b64_tr_b16 v[144:145], v235 offset:12288\n\t"
    "ds_read_b64_tr_b16 v[146:147], v235 offset:13312\n\t"
    "v_mfma_f32_32x32x16_bf16 v[176:191], a[140:143], v[12:15], v[176:191]\n\t"
    "s_add_u32 m0, s56, 4096\n\t"
    "s_xor_b32 s80, s64, 147712\n\t"
    "buffer_load_dwordx4 v240, s[32:35], s80 offen nt lds\n\t"
    "v_mul_f32 v164, v164, v216\n\t"
    "v_mul_f32 v165, v165, v216\n\t"
    "v_mul_f32 v166, v166, v216\n\t"
    "v_mul_f32 v167, v167, v216\n\t"
    "ds_read_b64_tr_b16 v[152:153], v236 offset:12288\n\t"
    "ds_read_b64_tr_b16 v[154:155], v236 offset:13312\n\t"
    "ds_read_b64_tr_b16 v[132:133], v244 offset:14336\n\t"
    "ds_read_b64_tr_b16 v[134:135], v244 offset:15360\n\t"
    "v_mfma_f32_32x32x16_bf16 v[176:191], a[148:151], v[20:23], v[176:191]\n\t"
    "v_mul_f32 v172, v172, v216\n\t"
    "v_mul_f32 v173, v173, v216\n\t"
    "v_mul_f32 v174, v174, v216\n\t"
    "v_mul_f32 v175, v175, v216\n\t"
    "ds_read_b64_tr_b16 v[140:141], v234 offset:14336\n\t"
    "ds_read_b64_tr_b16 v[142:143], v234 offset:15360\n\t"
    "ds_read_b64_tr_b16 v[148:149], v235 offset:14336\n\t"
    "v_mfma_f32_32x32x16_bf16 v[176:191], a[156:159], v[28:31], v[176:191]\n\t"
    "buffer_load_dwordx4 v241, s[32:35], s80 offen offset:1024 nt lds\n\t"
    "v_cvt_pk_bf16_f32 v164, v164, v165\n\t"
    "v_cvt_pk_bf16_f32 v165, v166, v167\n\t"
    "v_cvt_pk_bf16_f32 v166, v172, v173\n\t"
    "v_cvt_pk_bf16_f32 v167, v174, v175\n\t"
    "ds_write_b128 v237, v[164:167] offset:31232\n\t"
    "ds_read_b64_tr_b16 v[150:151], v235 offset:15360\n\t"
    "ds_read_b64_tr_b16 v[156:157], v236 offset:14336\n\t"
    "ds_read_b64_tr_b16 v[158:159], v236 offset:15360\n\t"
    "s_waitcnt lgkmcnt(14)\n\t"
    "v_mfma_f32_32x32x16_bf16 v[176:191], v[128:131], v[64:67], v[176:191]\n\t"
    "s_waitcnt lgkmcnt(13)\n\t"
    "v_mfma_f32_32x32x16_bf16 v[176:191], v[136:139], v[72:75], v[176:191]\n\t"
    "buffer_load_dwordx4 v242, s[32:35], s80 offen offset:2048 nt lds\n\t"
    "s_waitcnt lgkmcnt(11)\n\t"
    "v_mfma_f32_32x32x16_bf16 v[176:191], v[144:147], v[80:83], v[176:191]\n\t"
    "s_waitcnt lgkmcnt(9)\n\t"
    "v_mfma_f32_32x32x16_bf16 v[176:191], v[152:155], v[88:91], v[176:191]\n\t"
    "s_waitcnt vmcnt(11)\n\t"
    "s_waitcnt lgkmcnt(0)\n\t"
    "s_barrier\n\t"
    "ds_read_b64_tr_b16 a[128:129], v244 offset:16384\n\t"
    "ds_read_b64_tr_b16 a[130:131], v244 offset:17408\n\t"
    "ds_read_b64_tr_b16 a[136:137], v234 offset:16384\n\t"
    "v_mfma_f32_32x32x16_bf16 v[176:191], v[132:135], v[68:71], v[176:191]\n\t"
    "buffer_load_dwordx4 v243, s[32:35], s80 offen offset:3072 nt lds\n\t"
    "ds_read_b64_tr_b16 a[138:139], v234 offset:17408\n\t"
    "ds_read_b64_tr_b16 a[144:145], v235 offset:16384\n\t"
    "ds_read_b64_tr_b16 a[146:147], v235 offset:17408\n\t"
    "v_mfma_f32_32x32x16_bf16 v[176:191], v[140:143], v[76:79], v[176:191]\n\t"
    "ds_read_b64_tr_b16 a[152:153], v236 offset:16384\n\t"
    "ds_read_b64_tr_b16 a[154:155], v236 offset:17408\n\t"
    "ds_read_b64_tr_b16 a[132:133], v244 offset:18432\n\t"
    "ds_read_b64_tr_b16 a[134:135], v244 offset:19456\n\t"
    "v_mfma_f32_32x32x16_bf16 v[176:191], v[148:151], v[84:87], v[176:191]\n\t"
    "s_add_u32 m0, s56, 24576\n\t"
    "s_xor_b32 s80, s64, 24832\n\t"
    "buffer_load_dwordx4 v240, s[32:35], s80 offen nt lds\n\t"
    "ds_read_b64_tr_b16 a[140:141], v234 offset:18432\n\t"
    "ds_read_b64_tr_b16 a[142:143], v234 offset:19456\n\t"
    "ds_read_b64_tr_b16 a[148:149], v235 offset:18432\n\t"
    "v_mfma_f32_32x32x16_bf16 v[176:191], v[156:159], v[92:95], v[176:191]\n\t"
    "ds_read_b64_tr_b16 a[150:151], v235 offset:19456\n\t"
    "ds_read_b64_tr_b16 a[156:157], v236 offset:18432\n\t"
    "ds_read_b64_tr_b16 a[158:159], v236 offset:19456\n\t"
    "s_waitcnt lgkmcnt(14)\n\t"
    "v_mfma_f32_32x32x16_bf16 v[176:191], a[128:131], v[32:35], v[176:191]\n\t"
    "buffer_load_dwordx4 v241, s[32:35], s80 offen offset:1024 nt lds\n\t"
    "ds_read_b128 a[64:67], v254 offset:2048\n\t"
    "ds_read_b128 a[68:71], v254 offset:3072\n\t"
    "ds_read_b128 a[72:75], v254 offset:43008\n\t"
    "ds_read_b128 a[76:79], v254 offset:44032\n\t"
    "s_waitcnt lgkmcnt(14)\n\t"
    "v_mfma_f32_32x32x16_bf16 v[176:191], a[136:139], v[40:43], v[176:191]\n\t"
    "ds_read_b128 a[80:83], v255 offset:2048\n\t"
    "ds_read_b128 a[84:87], v255 offset:3072\n\t"
    "ds_read_b128 a[88:91], v255 offset:43008\n\t"
    "ds_read_b128 a[92:95], v255 offset:44032\n\t"
    "v_mfma_f32_32x32x16_bf16 v[176:191], a[144:147], v[48:51], v[176:191]\n\t"
    "s_waitcnt lgkmcnt(14)\n\t"
    "v_mfma_f32_32x32x16_bf16 v[176:191], a[152:155], v[56:59], v[176:191]\n\t"
    "buffer_load_dwordx4 v242, s[32:35], s80 offen offset:2048 nt lds\n\t"
    "buffer_load_dwordx4 v243, s[32:35], s80 offen offset:3072 nt lds\n\t"
    "s_waitcnt vmcnt(12)\n\t"
    "s_waitcnt lgkmcnt(0)\n\t"
    "s_barrier\n\t"
    "ds_read_b64_tr_b16 v[128:129], v244 offset:20480\n\t"
    "ds_read_b64_tr_b16 v[130:131], v244 offset:21504\n\t"
    "ds_read_b64_tr_b16 v[136:137], v234 offset:20480\n\t"
    "v_mfma_f32_32x32x16_bf16 v[176:191], a[132:135], v[36:39], v[176:191]\n\t"
    "ds_read_b64_tr_b16 v[138:139], v234 offset:21504\n\t"
    "ds_read_b64_tr_b16 v[144:145], v235 offset:20480\n\t"
    "ds_read_b64_tr_b16 v[146:147], v235 offset:21504\n\t"
    "v_mfma_f32_32x32x16_bf16 v[176:191], a[140:143], v[44:47], v[176:191]\n\t"
    "ds_read_b64_tr_b16 v[152:153], v236 offset:20480\n\t"
    "ds_read_b64_tr_b16 v[154:155], v236 offset:21504\n\t"
    "ds_read_b64_tr_b16 v[132:133], v244 offset:22528\n\t"
    "ds_read_b64_tr_b16 v[134:135], v244 offset:23552\n\t"
    "v_mfma_f32_32x32x16_bf16 v[176:191], a[148:151], v[52:55], v[176:191]\n\t"
    "s_add_u32 m0, s56, 32768\n\t"
    "s_xor_b32 s80, s64, 155904\n\t"
    "buffer_load_dwordx4 v240, s[32:35], s80 offen nt lds\n\t"
    "ds_read_b64_tr_b16 v[140:141], v234 offset:22528\n\t"
    "ds_read_b64_tr_b16 v[142:143], v234 offset:23552\n\t"
    "ds_read_b64_tr_b16 v[148:149], v235 offset:22528\n\t"
    "v_mfma_f32_32x32x16_bf16 v[176:191], a[156:159], v[60:63], v[176:191]\n\t"
    "ds_read_b64_tr_b16 v[150:151], v235 offset:23552\n\t"
    "ds_read_b64_tr_b16 v[156:157], v236 offset:22528\n\t"
    "ds_read_b64_tr_b16 v[158:159], v236 offset:23552\n\t"
    "s_waitcnt lgkmcnt(14)\n\t"
    "v_mfma_f32_32x32x16_bf16 v[176:191], v[128:131], v[96:99], v[176:191]\n\t"
    "s_waitcnt lgkmcnt(12)\n\t"
    "v_mfma_f32_32x32x16_bf16 v[176:191], v[136:139], a[160:163], v[176:191]\n\t"
    "buffer_load_dwordx4 v241, s[32:35], s80 offen offset:1024 nt lds\n\t"
    "s_waitcnt lgkmcnt(10)\n\t"
    "v_mfma_f32_32x32x16_bf16 v[176:191], v[144:147], a[168:171], v[176:191]\n\t"
    "s_waitcnt lgkmcnt(8)\n\t"
    "v_mfma_f32_32x32x16_bf16 v[176:191], v[152:155], a[176:179], v[176:191]\n\t"
    "buffer_load_dwordx4 v242, s[32:35], s80 offen offset:2048 nt lds\n\t"
    "buffer_load_dwordx4 v243, s[32:35], s80 offen offset:3072 nt lds\n\t"
    "s_waitcnt vmcnt(12)\n\t"
    "s_waitcnt lgkmcnt(0)\n\t"
    "s_barrier\n\t"
    "ds_read_b64_tr_b16 a[128:129], v244 offset:0\n\t"
    "ds_read_b64_tr_b16 a[130:131], v244 offset:1024\n\t"
    "ds_read_b64_tr_b16 a[136:137], v234 offset:0\n\t"
    "v_mfma_f32_32x32x16_bf16 v[176:191], v[132:135], v[100:103], v[176:191]\n\t"
    "ds_read_b64_tr_b16 a[138:139], v234 offset:1024\n\t"
    "ds_read_b64_tr_b16 a[144:145], v235 offset:0\n\t"
    "ds_read_b64_tr_b16 a[146:147], v235 offset:1024\n\t"
    "v_mfma_f32_32x32x16_bf16 v[176:191], v[140:143], a[164:167], v[176:191]\n\t"
    "ds_read_b64_tr_b16 a[152:153], v236 offset:0\n\t"
    "ds_read_b64_tr_b16 a[154:155], v236 offset:1024\n\t"
    "ds_read_b64_tr_b16 a[132:133], v244 offset:2048\n\t"
    "ds_read_b64_tr_b16 a[134:135], v244 offset:3072\n\t"
    "v_mfma_f32_32x32x16_bf16 v[176:191], v[148:151], a[172:175], v[176:191]\n\t"
    "s_add_u32 m0, s56, 36864\n\t"
    "s_xor_b32 s80, s64, 384\n\t"
    "buffer_load_dwordx4 v240, s[32:35], s80 offen nt lds\n\t"
    "ds_read_b64_tr_b16 a[140:141], v234 offset:2048\n\t"
    "ds_read_b64_tr_b16 a[142:143], v234 offset:3072\n\t"
    "ds_read_b64_tr_b16 a[148:149], v235 offset:2048\n\t"
    "v_mfma_f32_32x32x16_bf16 v[176:191], v[156:159], a[180:183], v[176:191]\n\t"
    "ds_read_b64_tr_b16 a[150:151], v235 offset:3072\n\t"
    "ds_read_b64_tr_b16 a[156:157], v236 offset:2048\n\t"
    "ds_read_b64_tr_b16 a[158:159], v236 offset:3072\n\t"
    "s_waitcnt lgkmcnt(14)\n\t"
    "v_mfma_f32_32x32x16_bf16 v[176:191], a[128:131], v[104:107], v[176:191]\n\t"
    "s_waitcnt lgkmcnt(12)\n\t"
    "v_mfma_f32_32x32x16_bf16 v[176:191], a[136:139], a[184:187], v[176:191]\n\t"
    "buffer_load_dwordx4 v241, s[32:35], s80 offen offset:1024 nt lds\n\t"
    "s_waitcnt lgkmcnt(10)\n\t"
    "v_mfma_f32_32x32x16_bf16 v[176:191], a[144:147], a[192:195], v[176:191]\n\t"
    "s_waitcnt lgkmcnt(8)\n\t"
    "v_mfma_f32_32x32x16_bf16 v[176:191], a[152:155], a[200:203], v[176:191]\n\t"
    "buffer_load_dwordx4 v242, s[32:35], s80 offen offset:2048 nt lds\n\t"
    "buffer_load_dwordx4 v243, s[32:35], s80 offen offset:3072 nt lds\n\t"
    "s_waitcnt vmcnt(12)\n\t"
    "s_waitcnt lgkmcnt(0)\n\t"
    "s_barrier\n\t"
    "ds_read_b64_tr_b16 v[128:129], v244 offset:4096\n\t"
    "ds_read_b64_tr_b16 v[130:131], v244 offset:5120\n\t"
    "ds_read_b64_tr_b16 v[136:137], v234 offset:4096\n\t"
    "v_mfma_f32_32x32x16_bf16 v[176:191], a[132:135], v[108:111], v[176:191]\n\t"
    "ds_read_b64_tr_b16 v[138:139], v234 offset:5120\n\t"
    "ds_read_b64_tr_b16 v[144:145], v235 offset:4096\n\t"
    "ds_read_b64_tr_b16 v[146:147], v235 offset:5120\n\t"
    "v_mfma_f32_32x32x16_bf16 v[176:191], a[140:143], a[188:191], v[176:191]\n\t"
    "ds_read_b64_tr_b16 v[152:153], v236 offset:4096\n\t"
    "ds_read_b64_tr_b16 v[154:155], v236 offset:5120\n\t"
    "ds_read_b64_tr_b16 v[132:133], v244 offset:6144\n\t"
    "ds_read_b64_tr_b16 v[134:135], v244 offset:7168\n\t"
    "v_mfma_f32_32x32x16_bf16 v[176:191], a[148:151], a[196:199], v[176:191]\n\t"
    "s_add_u32 m0, s56, 8192\n\t"
    "s_xor_b32 s80, s64, 131456\n\t"
    "buffer_load_dwordx4 v240, s[32:35], s80 offen nt lds\n\t"
    "ds_read_b64_tr_b16 v[140:141], v234 offset:6144\n\t"
    "ds_read_b64_tr_b16 v[142:143], v234 offset:7168\n\t"
    "ds_read_b64_tr_b16 v[148:149], v235 offset:6144\n\t"
    "v_mfma_f32_32x32x16_bf16 v[176:191], a[156:159], a[204:207], v[176:191]\n\t"
    "ds_read_b64_tr_b16 v[150:151], v235 offset:7168\n\t"
    "ds_read_b64_tr_b16 v[156:157], v236 offset:6144\n\t"
    "ds_read_b64_tr_b16 v[158:159], v236 offset:7168\n\t"
    "s_waitcnt lgkmcnt(14)\n\t"
    "v_mfma_f32_32x32x16_bf16 v[176:191], v[128:131], v[120:123], v[176:191]\n\t"
    "s_waitcnt lgkmcnt(12)\n\t"
    "v_mfma_f32_32x32x16_bf16 v[176:191], v[136:139], a[232:235], v[176:191]\n\t"
    "buffer_load_dwordx4 v241, s[32:35], s80 offen offset:1024 nt lds\n\t"
    "s_waitcnt lgkmcnt(10)\n\t"
    "v_mfma_f32_32x32x16_bf16 v[176:191], v[144:147], a[240:243], v[176:191]\n\t"
    "s_waitcnt lgkmcnt(8)\n\t"
    "v_mfma_f32_32x32x16_bf16 v[176:191], v[152:155], a[248:251], v[176:191]\n\t"
    "buffer_load_dwordx4 v242, s[32:35], s80 offen offset:2048 nt lds\n\t"
    "buffer_load_dwordx4 v243, s[32:35], s80 offen offset:3072 nt lds\n\t"
    "s_waitcnt vmcnt(12)\n\t"
    "s_waitcnt lgkmcnt(0)\n\t"
    "s_barrier\n\t"
    "ds_read_b64_tr_b16 a[128:129], v244 offset:24576\n\t"
    "ds_read_b64_tr_b16 a[130:131], v244 offset:25600\n\t"
    "ds_read_b64_tr_b16 a[136:137], v234 offset:24576\n\t"
    "v_mfma_f32_32x32x16_bf16 v[176:191], v[132:135], v[124:127], v[176:191]\n\t"
    "ds_read_b64_tr_b16 a[138:139], v234 offset:25600\n\t"
    "ds_read_b64_tr_b16 a[144:145], v235 offset:24576\n\t"
    "ds_read_b64_tr_b16 a[146:147], v235 offset:25600\n\t"
    "v_mfma_f32_32x32x16_bf16 v[176:191], v[140:143], a[236:239], v[176:191]\n\t"
    "ds_read_b64_tr_b16 a[152:153], v236 offset:24576\n\t"
    "ds_read_b64_tr_b16 a[154:155], v236 offset:25600\n\t"
    "ds_read_b64_tr_b16 a[132:133], v244 offset:26624\n\t"
    "ds_read_b64_tr_b16 a[134:135], v244 offset:27648\n\t"
    "v_mfma_f32_32x32x16_bf16 v[176:191], v[148:151], a[244:247], v[176:191]\n\t"
    "s_add_u32 m0, s56, 12288\n\t"
    "s_xor_b32 s80, s64, 8576\n\t"
    "buffer_load_dwordx4 v240, s[32:35], s80 offen nt lds\n\t"
    "ds_read_b64_tr_b16 a[140:141], v234 offset:26624\n\t"
    "ds_read_b64_tr_b16 a[142:143], v234 offset:27648\n\t"
    "ds_read_b64_tr_b16 a[148:149], v235 offset:26624\n\t"
    "v_mfma_f32_32x32x16_bf16 v[176:191], v[156:159], a[252:255], v[176:191]\n\t"
    "ds_read_b64_tr_b16 a[150:151], v235 offset:27648\n\t"
    "ds_read_b64_tr_b16 a[156:157], v236 offset:26624\n\t"
    "ds_read_b64_tr_b16 a[158:159], v236 offset:27648\n\t"
    "s_waitcnt lgkmcnt(14)\n\t"
    "v_mfma_f32_32x32x16_bf16 v[176:191], a[128:131], v[112:115], v[176:191]\n\t"
    "s_waitcnt lgkmcnt(12)\n\t"
    "v_mfma_f32_32x32x16_bf16 v[176:191], a[136:139], a[208:211], v[176:191]\n\t"
    "buffer_load_dwordx4 v241, s[32:35], s80 offen offset:1024 nt lds\n\t"
    "s_waitcnt lgkmcnt(10)\n\t"
    "v_mfma_f32_32x32x16_bf16 v[176:191], a[144:147], a[216:219], v[176:191]\n\t"
    "s_waitcnt lgkmcnt(8)\n\t"
    "v_mfma_f32_32x32x16_bf16 v[176:191], a[152:155], a[224:227], v[176:191]\n\t"
    "buffer_load_dwordx4 v242, s[32:35], s80 offen offset:2048 nt lds\n\t"
    "buffer_load_dwordx4 v243, s[32:35], s80 offen offset:3072 nt lds\n\t"
    "s_waitcnt vmcnt(12)\n\t"
    "s_waitcnt lgkmcnt(0)\n\t"
    "s_barrier\n\t"
    "ds_read_b64_tr_b16 v[128:129], v244 offset:32768\n\t"
    "ds_read_b64_tr_b16 v[130:131], v244 offset:33792\n\t"
    "ds_read_b64_tr_b16 v[136:137], v234 offset:32768\n\t"
    "v_mfma_f32_32x32x16_bf16 v[176:191], a[132:135], v[116:119], v[176:191]\n\t"
    "ds_read_b64_tr_b16 v[138:139], v234 offset:33792\n\t"
    "ds_read_b64_tr_b16 v[144:145], v235 offset:32768\n\t"
    "ds_read_b64_tr_b16 v[146:147], v235 offset:33792\n\t"
    "v_mfma_f32_32x32x16_bf16 v[176:191], a[140:143], a[212:215], v[176:191]\n\t"
    "ds_read_b64_tr_b16 v[152:153], v236 offset:32768\n\t"
    "ds_read_b64_tr_b16 v[154:155], v236 offset:33792\n\t"
    "ds_read_b64_tr_b16 v[132:133], v244 offset:34816\n\t"
    "ds_read_b64_tr_b16 v[134:135], v244 offset:35840\n\t"
    "v_mfma_f32_32x32x16_bf16 v[176:191], a[148:151], a[220:223], v[176:191]\n\t"
    "s_add_u32 m0, s56, 16384\n\t"
    "s_xor_b32 s80, s64, 139648\n\t"
    "buffer_load_dwordx4 v240, s[32:35], s80 offen nt lds\n\t"
    "ds_read_b64_tr_b16 v[140:141], v234 offset:34816\n\t"
    "ds_read_b64_tr_b16 v[142:143], v234 offset:35840\n\t"
    "ds_read_b64_tr_b16 v[148:149], v235 offset:34816\n\t"
    "v_mfma_f32_32x32x16_bf16 v[176:191], a[156:159], a[228:231], v[176:191]\n\t"
    "ds_read_b64_tr_b16 v[150:151], v235 offset:35840\n\t"
    "ds_read_b64_tr_b16 v[156:157], v236 offset:34816\n\t"
    "ds_read_b64_tr_b16 v[158:159], v236 offset:35840\n\t"
    "s_waitcnt lgkmcnt(14)\n\t"
    "v_mfma_f32_32x32x16_bf16 v[176:191], v[128:131], v[200:203], v[176:191]\n\t"
    "s_waitcnt lgkmcnt(12)\n\t"
    "v_mfma_f32_32x32x16_bf16 v[176:191], v[136:139], a[0:3], v[176:191]\n\t"
    "buffer_load_dwordx4 v241, s[32:35], s80 offen offset:1024 nt lds\n\t"
    "s_waitcnt lgkmcnt(10)\n\t"
    "v_mfma_f32_32x32x16_bf16 v[176:191], v[144:147], a[8:11], v[176:191]\n\t"
    "s_waitcnt lgkmcnt(8)\n\t"
    "v_mfma_f32_32x32x16_bf16 v[176:191], v[152:155], a[16:19], v[176:191]\n\t"
    "buffer_load_dwordx4 v242, s[32:35], s80 offen offset:2048 nt lds\n\t"
    "buffer_load_dwordx4 v243, s[32:35], s80 offen offset:3072 nt lds\n\t"
    "s_waitcnt vmcnt(12)\n\t"
    "s_waitcnt lgkmcnt(0)\n\t"
    "s_barrier\n\t"
    "ds_read_b64_tr_b16 a[128:129], v244 offset:36864\n\t"
    "ds_read_b64_tr_b16 a[130:131], v244 offset:37888\n\t"
    "ds_read_b64_tr_b16 a[136:137], v234 offset:36864\n\t"
    "v_mfma_f32_32x32x16_bf16 v[176:191], v[132:135], v[204:207], v[176:191]\n\t"
    "ds_read_b64_tr_b16 a[138:139], v234 offset:37888\n\t"
    "ds_read_b64_tr_b16 a[144:145], v235 offset:36864\n\t"
    "ds_read_b64_tr_b16 a[146:147], v235 offset:37888\n\t"
    "v_mfma_f32_32x32x16_bf16 v[176:191], v[140:143], a[4:7], v[176:191]\n\t"
    "ds_read_b64_tr_b16 a[152:153], v236 offset:36864\n\t"
    "ds_read_b64_tr_b16 a[154:155], v236 offset:37888\n\t"
    "ds_read_b64_tr_b16 a[132:133], v244 offset:38912\n\t"
    "ds_read_b64_tr_b16 a[134:135], v244 offset:39936\n\t"
    "v_mfma_f32_32x32x16_bf16 v[176:191], v[148:151], a[12:15], v[176:191]\n\t"
    "s_add_u32 m0, s56, 20480\n\t"
    "s_lshr_b32 s80, s57, 12\n\t"
    "buffer_load_dwordx4 v246, s[36:39], s80 offen lds\n\t"
    "ds_read_b64_tr_b16 a[140:141], v234 offset:38912\n\t"
    "ds_read_b64_tr_b16 a[142:143], v234 offset:39936\n\t"
    "ds_read_b64_tr_b16 a[148:149], v235 offset:38912\n\t"
    "v_mfma_f32_32x32x16_bf16 v[176:191], v[156:159], a[20:23], v[176:191]\n\t"
    "ds_read_b64_tr_b16 a[150:151], v235 offset:39936\n\t"
    "ds_read_b64_tr_b16 a[156:157], v236 offset:38912\n\t"
    "ds_read_b64_tr_b16 a[158:159], v236 offset:39936\n\t"
    "s_waitcnt lgkmcnt(14)\n\t"
    "v_mfma_f32_32x32x16_bf16 v[160:175], a[128:131], v[0:3], 0\n\t"
    "s_waitcnt lgkmcnt(12)\n\t"
    "v_mfma_f32_32x32x16_bf16 v[160:175], a[136:139], v[8:11], v[160:175]\n\t"
    "buffer_load_dwordx4 v247, s[36:39], s80 offen offset:1024 lds\n\t"
    "s_waitcnt lgkmcnt(10)\n\t"
    "v_mfma_f32_32x32x16_bf16 v[160:175], a[144:147], v[16:19], v[160:175]\n\t"
    "s_nop 1\n\t"
    "v_mul_f32 v176, v176, v216\n\t"
    "v_mul_f32 v177, v177, v216\n\t"
    "v_mul_f32 v178, v178, v216\n\t"
    "v_mul_f32 v179, v179, v216\n\t"
    "s_waitcnt lgkmcnt(8)\n\t"
    "v_mfma_f32_32x32x16_bf16 v[160:175], a[152:155], v[24:27], v[160:175]\n\t"
    "v_mul_f32 v184, v184, v216\n\t"
    "v_mul_f32 v185, v185, v216\n\t"
    "v_mul_f32 v186, v186, v216\n\t"
    "v_mul_f32 v187, v187, v216\n\t"
    "buffer_load_dwordx4 v248, s[36:39], s80 offen offset:2048 lds\n\t"
    "buffer_load_dwordx4 v249, s[36:39], s80 offen offset:3072 lds\n\t"
    "s_waitcnt vmcnt(12)\n\t"
    "s_waitcnt lgkmcnt(0)\n\t"
    "s_barrier\n\t"
    "ds_read_b64_tr_b16 v[128:129], v244 offset:8192\n\t"
    "ds_read_b64_tr_b16 v[130:131], v244 offset:9216\n\t"
    "ds_read_b64_tr_b16 v[136:137], v234 offset:8192\n\t"
    "v_mfma_f32_32x32x16_bf16 v[160:175], a[132:135], v[4:7], v[160:175]\n\t"
    "v_cvt_pk_bf16_f32 v176, v176, v177\n\t"
    "v_cvt_pk_bf16_f32 v177, v178, v179\n\t"
    "v_cvt_pk_bf16_f32 v178, v184, v185\n\t"
    "v_cvt_pk_bf16_f32 v179, v186, v187\n\t"
    "ds_write_b128 v237, v[176:179] offset:28672\n\t"
    "ds_read_b64_tr_b16 v[138:139], v234 offset:9216\n\t"
    "ds_read_b64_tr_b16 v[144:145], v235 offset:8192\n\t"
    "ds_read_b64_tr_b16 v[146:147], v235 offset:9216\n\t"
    "v_mfma_f32_32x32x16_bf16 v[160:175], a[140:143], v[12:15], v[160:175]\n\t"
    "s_add_u32 m0, s56, 0\n\t"
    "s_lshr_b32 s80, s57, 12\n\t"
    "s_add_u32 s80, s80, 28672\n\t"
    "buffer_load_dwordx4 v246, s[36:39], s80 offen lds\n\t"
    "v_mul_f32 v180, v180, v216\n\t"
    "v_mul_f32 v181, v181, v216\n\t"
    "v_mul_f32 v182, v182, v216\n\t"
    "v_mul_f32 v183, v183, v216\n\t"
    "ds_read_b64_tr_b16 v[152:153], v236 offset:8192\n\t"
    "ds_read_b64_tr_b16 v[154:155], v236 offset:9216\n\t"
    "ds_read_b64_tr_b16 v[132:133], v244 offset:10240\n\t"
    "ds_read_b64_tr_b16 v[134:135], v244 offset:11264\n\t"
    "v_mfma_f32_32x32x16_bf16 v[160:175], a[148:151], v[20:23], v[160:175]\n\t"
    "v_mul_f32 v188, v188, v216\n\t"
    "v_mul_f32 v189, v189, v216\n\t"
    "v_mul_f32 v190, v190, v216\n\t"
    "v_mul_f32 v191, v191, v216\n\t"
    "ds_read_b64_tr_b16 v[140:141], v234 offset:10240\n\t"
    "ds_read_b64_tr_b16 v[142:143], v234 offset:11264\n\t"
    "ds_read_b64_tr_b16 v[148:149], v235 offset:10240\n\t"
    "v_mfma_f32_32x32x16_bf16 v[160:175], a[156:159], v[28:31], v[160:175]\n\t"
    "buffer_load_dwordx4 v247, s[36:39], s80 offen offset:1024 lds\n\t"
    "v_cvt_pk_bf16_f32 v180, v180, v181\n\t"
    "v_cvt_pk_bf16_f32 v181, v182, v183\n\t"
    "v_cvt_pk_bf16_f32 v182, v188, v189\n\t"
    "v_cvt_pk_bf16_f32 v183, v190, v191\n\t"
    "ds_write_b128 v237, v[180:183] offset:29184\n\t"
    "ds_read_b64_tr_b16 v[150:151], v235 offset:11264\n\t"
    "ds_read_b64_tr_b16 v[156:157], v236 offset:10240\n\t"
    "ds_read_b64_tr_b16 v[158:159], v236 offset:11264\n\t"
    "s_waitcnt lgkmcnt(14)\n\t"
    "v_mfma_f32_32x32x16_bf16 v[160:175], v[128:131], v[64:67], v[160:175]\n\t"
    "s_waitcnt lgkmcnt(13)\n\t"
    "v_mfma_f32_32x32x16_bf16 v[160:175], v[136:139], v[72:75], v[160:175]\n\t"
    "buffer_load_dwordx4 v248, s[36:39], s80 offen offset:2048 lds\n\t"
    "s_waitcnt lgkmcnt(11)\n\t"
    "v_mfma_f32_32x32x16_bf16 v[160:175], v[144:147], v[80:83], v[160:175]\n\t"
    "s_waitcnt lgkmcnt(9)\n\t"
    "v_mfma_f32_32x32x16_bf16 v[160:175], v[152:155], v[88:91], v[160:175]\n\t"
    "s_waitcnt vmcnt(11)\n\t"
    "s_waitcnt lgkmcnt(0)\n\t"
    "s_barrier\n\t"
    "ds_read_b64_tr_b16 a[128:129], v244 offset:12288\n\t"
    "ds_read_b64_tr_b16 a[130:131], v244 offset:13312\n\t"
    "ds_read_b64_tr_b16 a[136:137], v234 offset:12288\n\t"
    "v_mfma_f32_32x32x16_bf16 v[160:175], v[132:135], v[68:71], v[160:175]\n\t"
    "buffer_load_dwordx4 v249, s[36:39], s80 offen offset:3072 lds\n\t"
    "ds_read_b64_tr_b16 a[138:139], v234 offset:13312\n\t"
    "ds_read_b64_tr_b16 a[144:145], v235 offset:12288\n\t"
    "ds_read_b64_tr_b16 a[146:147], v235 offset:13312\n\t"
    "v_mfma_f32_32x32x16_bf16 v[160:175], v[140:143], v[76:79], v[160:175]\n\t"
    "ds_read_b64_tr_b16 a[152:153], v236 offset:12288\n\t"
    "ds_read_b64_tr_b16 a[154:155], v236 offset:13312\n\t"
    "ds_read_b64_tr_b16 a[132:133], v244 offset:14336\n\t"
    "ds_read_b64_tr_b16 a[134:135], v244 offset:15360\n\t"
    "v_mfma_f32_32x32x16_bf16 v[160:175], v[148:151], v[84:87], v[160:175]\n\t"
    "s_add_u32 m0, s56, 4096\n\t"
    "s_xor_b32 s80, s64, 16768\n\t"
    "buffer_load_dwordx4 v240, s[32:35], s80 offen nt lds\n\t"
    "ds_read_b64_tr_b16 a[140:141], v234 offset:14336\n\t"
    "ds_read_b64_tr_b16 a[142:143], v234 offset:15360\n\t"
    "ds_read_b64_tr_b16 a[148:149], v235 offset:14336\n\t"
    "v_mfma_f32_32x32x16_bf16 v[160:175], v[156:159], v[92:95], v[160:175]\n\t"
    "ds_read_b64_tr_b16 a[150:151], v235 offset:15360\n\t"
    "ds_read_b64_tr_b16 a[156:157], v236 offset:14336\n\t"
    "ds_read_b64_tr_b16 a[158:159], v236 offset:15360\n\t"
    "s_waitcnt lgkmcnt(14)\n\t"
    "v_mfma_f32_32x32x16_bf16 v[160:175], a[128:131], v[32:35], v[160:175]\n\t"
    "buffer_load_dwordx4 v241, s[32:35], s80 offen offset:1024 nt lds\n\t"
    "ds_read_b128 a[96:99], v254 offset:0\n\t"
    "ds_read_b128 a[100:103], v254 offset:1024\n\t"
    "ds_read_b128 a[104:107], v254 offset:40960\n\t"
    "ds_read_b128 a[108:111], v254 offset:41984\n\t"
    "s_waitcnt lgkmcnt(14)\n\t"
    "v_mfma_f32_32x32x16_bf16 v[160:175], a[136:139], v[40:43], v[160:175]\n\t"
    "ds_read_b128 a[112:115], v255 offset:0\n\t"
    "ds_read_b128 a[116:119], v255 offset:1024\n\t"
    "ds_read_b128 a[120:123], v255 offset:40960\n\t"
    "ds_read_b128 a[124:127], v255 offset:41984\n\t"
    "v_mfma_f32_32x32x16_bf16 v[160:175], a[144:147], v[48:51], v[160:175]\n\t"
    "s_waitcnt lgkmcnt(14)\n\t"
    "v_mfma_f32_32x32x16_bf16 v[160:175], a[152:155], v[56:59], v[160:175]\n\t"
    "buffer_load_dwordx4 v242, s[32:35], s80 offen offset:2048 nt lds\n\t"
    "buffer_load_dwordx4 v243, s[32:35], s80 offen offset:3072 nt lds\n\t"
    "s_waitcnt vmcnt(12)\n\t"
    "s_waitcnt lgkmcnt(0)\n\t"
    "s_barrier\n\t"
    "ds_read_b64_tr_b16 v[128:129], v244 offset:16384\n\t"
    "ds_read_b64_tr_b16 v[130:131], v244 offset:17408\n\t"
    "ds_read_b64_tr_b16 v[136:137], v234 offset:16384\n\t"
    "v_mfma_f32_32x32x16_bf16 v[160:175], a[132:135], v[36:39], v[160:175]\n\t"
    "ds_read_b64_tr_b16 v[138:139], v234 offset:17408\n\t"
    "ds_read_b64_tr_b16 v[144:145], v235 offset:16384\n\t"
    "ds_read_b64_tr_b16 v[146:147], v235 offset:17408\n\t"
    "v_mfma_f32_32x32x16_bf16 v[160:175], a[140:143], v[44:47], v[160:175]\n\t"
    "ds_read_b64_tr_b16 v[152:153], v236 offset:16384\n\t"
    "ds_read_b64_tr_b16 v[154:155], v236 offset:17408\n\t"
    "ds_read_b64_tr_b16 v[132:133], v244 offset:18432\n\t"
    "ds_read_b64_tr_b16 v[134:135], v244 offset:19456\n\t"
    "v_mfma_f32_32x32x16_bf16 v[160:175], a[148:151], v[52:55], v[160:175]\n\t"
    "s_add_u32 m0, s56, 24576\n\t"
    "s_xor_b32 s80, s64, 147840\n\t"
    "buffer_load_dwordx4 v240, s[32:35], s80 offen nt lds\n\t"
    "ds_read_b64_tr_b16 v[140:141], v234 offset:18432\n\t"
    "ds_read_b64_tr_b16 v[142:143], v234 offset:19456\n\t"
    "ds_read_b64_tr_b16 v[148:149], v235 offset:18432\n\t"
    "v_mfma_f32_32x32x16_bf16 v[160:175], a[156:159], v[60:63], v[160:175]\n\t"
    "ds_read_b64_tr_b16 v[150:151], v235 offset:19456\n\t"
    "ds_read_b64_tr_b16 v[156:157], v236 offset:18432\n\t"
    "ds_read_b64_tr_b16 v[158:159], v236 offset:19456\n\t"
    "s_waitcnt lgkmcnt(14)\n\t"
    "v_mfma_f32_32x32x16_bf16 v[160:175], v[128:131], v[96:99], v[160:175]\n\t"
    "s_waitcnt lgkmcnt(12)\n\t"
    "v_mfma_f32_32x32x16_bf16 v[160:175], v[136:139], a[160:163], v[160:175]\n\t"
    "buffer_load_dwordx4 v241, s[32:35], s80 offen offset:1024 nt lds\n\t"
    "s_waitcnt lgkmcnt(10)\n\t"
    "v_mfma_f32_32x32x16_bf16 v[160:175], v[144:147], a[168:171], v[160:175]\n\t"
    "s_waitcnt lgkmcnt(8)\n\t"
    "v_mfma_f32_32x32x16_bf16 v[160:175], v[152:155], a[176:179], v[160:175]\n\t"
    "buffer_load_dwordx4 v242, s[32:35], s80 offen offset:2048 nt lds\n\t"
    "buffer_load_dwordx4 v243, s[32:35], s80 offen offset:3072 nt lds\n\t"
    "s_add_u32 m0, s56, 32768\n\t"
    "s_xor_b32 s80, s64, 24960\n\t"
    "buffer_load_dwordx4 v240, s[32:35], s80 offen nt lds\n\t"
    "buffer_load_dwordx4 v241, s[32:35], s80 offen offset:1024 nt lds\n\t"
    "buffer_load_dwordx4 v242, s[32:35], s80 offen offset:2048 nt lds\n\t"
    "buffer_load_dwordx4 v243, s[32:35], s80 offen offset:3072 nt lds\n\t"
    "s_add_u32 m0, s56, 36864\n\t"
    "s_xor_b32 s80, s64, 156032\n\t"
    "buffer_load_dwordx4 v240, s[32:35], s80 offen nt lds\n\t"
    "buffer_load_dwordx4 v241, s[32:35], s80 offen offset:1024 nt lds\n\t"
    "buffer_load_dwordx4 v242, s[32:35], s80 offen offset:2048 nt lds\n\t"
    "buffer_load_dwordx4 v243, s[32:35], s80 offen offset:3072 nt lds\n\t"
    "s_mov_b64 exec, 0\n\t"
    "s_cmp_eq_u32 s70, 0\n\t"
    "s_cbranch_scc1 LT0_0_%=\n\t"
    "s_cmp_eq_u32 s70, 1\n\t"
    "s_cbranch_scc1 LT1_0_%=\n\t"
    "s_cmp_eq_u32 s70, 2\n\t"
    "s_cbranch_scc1 LT2_0_%=\n\t"
    "s_branch LT3_0_%=\n\t"
    "LTr_%=:\n\t"
    "s_mov_b64 exec, -1\n\t"
    "s_waitcnt vmcnt(12)\n\t"
    "s_waitcnt lgkmcnt(0)\n\t"
    "s_barrier\n\t"
    "ds_read_b64_tr_b16 a[128:129], v244 offset:4096\n\t"
    "ds_read_b64_tr_b16 a[130:131], v244 offset:5120\n\t"
    "ds_read_b64_tr_b16 a[136:137], v234 offset:4096\n\t"
    "v_mfma_f32_32x32x16_bf16 v[160:175], v[132:135], v[100:103], v[160:175]\n\t"
    "ds_read_b64_tr_b16 a[138:139], v234 offset:5120\n\t"
    "ds_read_b64_tr_b16 a[144:145], v235 offset:4096\n\t"
    "ds_read_b64_tr_b16 a[146:147], v235 offset:5120\n\t"
    "v_mfma_f32_32x32x16_bf16 v[160:175], v[140:143], a[164:167], v[160:175]\n\t"
    "ds_read_b64_tr_b16 a[152:153], v236 offset:4096\n\t"
    "ds_read_b64_tr_b16 a[154:155], v236 offset:5120\n\t"
    "ds_read_b64_tr_b16 a[132:133], v244 offset:6144\n\t"
    "ds_read_b64_tr_b16 a[134:135], v244 offset:7168\n\t"
    "v_mfma_f32_32x32x16_bf16 v[160:175], v[148:151], a[172:175], v[160:175]\n\t"
    "s_add_u32 m0, s56, 8192\n\t"
    "s_lshr_b32 s80, s58, 12\n\t"
    "buffer_load_dwordx4 v246, s[36:39], s80 offen lds\n\t"
    "ds_read_b64_tr_b16 a[140:141], v234 offset:6144\n\t"
    "ds_read_b64_tr_b16 a[142:143], v234 offset:7168\n\t"
    "ds_read_b64_tr_b16 a[148:149], v235 offset:6144\n\t"
    "v_mfma_f32_32x32x16_bf16 v[160:175], v[156:159], a[180:183], v[160:175]\n\t"
    "ds_read_b64_tr_b16 a[150:151], v235 offset:7168\n\t"
    "ds_read_b64_tr_b16 a[156:157], v236 offset:6144\n\t"
    "ds_read_b64_tr_b16 a[158:159], v236 offset:7168\n\t"
    "s_waitcnt lgkmcnt(14)\n\t"
    "v_mfma_f32_32x32x16_bf16 v[160:175], a[128:131], v[104:107], v[160:175]\n\t"
    "s_waitcnt lgkmcnt(12)\n\t"
    "v_mfma_f32_32x32x16_bf16 v[160:175], a[136:139], a[184:187], v[160:175]\n\t"
    "buffer_load_dwordx4 v247, s[36:39], s80 offen offset:1024 lds\n\t"
    "s_waitcnt lgkmcnt(10)\n\t"
    "v_mfma_f32_32x32x16_bf16 v[160:175], a[144:147], a[192:195], v[160:175]\n\t"
    "s_waitcnt lgkmcnt(8)\n\t"
    "v_mfma_f32_32x32x16_bf16 v[160:175], a[152:155], a[200:203], v[160:175]\n\t"
    "buffer_load_dwordx4 v248, s[36:39], s80 offen offset:2048 lds\n\t"
    "buffer_load_dwordx4 v249, s[36:39], s80 offen offset:3072 lds\n\t"
    "s_waitcnt vmcnt(12)\n\t"
    "s_waitcnt lgkmcnt(0)\n\t"
    "s_barrier\n\t"
    "ds_read_b64_tr_b16 v[128:129], v244 offset:24576\n\t"
    "ds_read_b64_tr_b16 v[130:131], v244 offset:25600\n\t"
    "ds_read_b64_tr_b16 v[136:137], v234 offset:24576\n\t"
    "v_mfma_f32_32x32x16_bf16 v[160:175], a[132:135], v[108:111], v[160:175]\n\t"
    "ds_read_b64_tr_b16 v[138:139], v234 offset:25600\n\t"
    "ds_read_b64_tr_b16 v[144:145], v235 offset:24576\n\t"
    "ds_read_b64_tr_b16 v[146:147], v235 offset:25600\n\t"
    "v_mfma_f32_32x32x16_bf16 v[160:175], a[140:143], a[188:191], v[160:175]\n\t"
    "ds_read_b64_tr_b16 v[152:153], v236 offset:24576\n\t"
    "ds_read_b64_tr_b16 v[154:155], v236 offset:25600\n\t"
    "ds_read_b64_tr_b16 v[132:133], v244 offset:26624\n\t"
    "ds_read_b64_tr_b16 v[134:135], v244 offset:27648\n\t"
    "v_mfma_f32_32x32x16_bf16 v[160:175], a[148:151], a[196:199], v[160:175]\n\t"
    "s_add_u32 m0, s56, 12288\n\t"
    "s_lshr_b32 s80, s58, 12\n\t"
    "s_add_u32 s80, s80, 28672\n\t"
    "buffer_load_dwordx4 v246, s[36:39], s80 offen lds\n\t"
    "ds_read_b64_tr_b16 v[140:141], v234 offset:26624\n\t"
    "ds_read_b64_tr_b16 v[142:143], v234 offset:27648\n\t"
    "ds_read_b64_tr_b16 v[148:149], v235 offset:26624\n\t"
    "v_mfma_f32_32x32x16_bf16 v[160:175], a[156:159], a[204:207], v[160:175]\n\t"
    "ds_read_b64_tr_b16 v[150:151], v235 offset:27648\n\t"
    "ds_read_b64_tr_b16 v[156:157], v236 offset:26624\n\t"
    "ds_read_b64_tr_b16 v[158:159], v236 offset:27648\n\t"
    "LT0_0_%=:\n\t"
    "s_cbranch_execz LT0_1_%=\n\t"
    "s_waitcnt lgkmcnt(14)\n\t"
    "v_mfma_f32_32x32x16_bf16 v[160:175], v[128:131], v[120:123], v[160:175]\n\t"
    "buffer_load_dwordx4 v247, s[36:39], s80 offen offset:1024 lds\n\t"
    "s_waitcnt lgkmcnt(12)\n\t"
    "v_mfma_f32_32x32x16_bf16 v[160:175], v[136:139], a[232:235], v[160:175]\n\t"
    "s_waitcnt lgkmcnt(10)\n\t"
    "v_mfma_f32_32x32x16_bf16 v[160:175], v[144:147], a[240:243], v[160:175]\n\t"
    "s_waitcnt lgkmcnt(8)\n\t"
    "v_mfma_f32_32x32x16_bf16 v[160:175], v[152:155], a[248:251], v[160:175]\n\t"
    "buffer_load_dwordx4 v248, s[36:39], s80 offen offset:2048 lds\n\t"
    "buffer_load_dwordx4 v249, s[36:39], s80 offen offset:3072 lds\n\t"
    "s_waitcnt vmcnt(12)\n\t"
    "s_waitcnt lgkmcnt(0)\n\t"
    "s_barrier\n\t"
    "ds_read_b64_tr_b16 a[128:129], v244 offset:32768\n\t"
    "ds_read_b64_tr_b16 a[130:131], v244 offset:33792\n\t"
    "ds_read_b64_tr_b16 a[136:137], v234 offset:32768\n\t"
    "v_mfma_f32_32x32x16_bf16 v[160:175], v[132:135], v[124:127], v[160:175]\n\t"
    "ds_read_b64_tr_b16 a[138:139], v234 offset:33792\n\t"
    "ds_read_b64_tr_b16 a[144:145], v235 offset:32768\n\t"
    "ds_read_b64_tr_b16 a[146:147], v235 offset:33792\n\t"
    "v_mfma_f32_32x32x16_bf16 v[160:175], v[140:143], a[236:239], v[160:175]\n\t"
    "ds_read_b64_tr_b16 a[152:153], v236 offset:32768\n\t"
    "ds_read_b64_tr_b16 a[154:155], v236 offset:33792\n\t"
    "ds_read_b64_tr_b16 a[132:133], v244 offset:34816\n\t"
    "ds_read_b64_tr_b16 a[134:135], v244 offset:35840\n\t"
    "v_mfma_f32_32x32x16_bf16 v[160:175], v[148:151], a[244:247], v[160:175]\n\t"
    "s_add_u32 m0, s56, 16384\n\t"
    "s_lshr_b32 s80, s59, 12\n\t"
    "buffer_load_dwordx4 v246, s[36:39], s80 offen lds\n\t"
    "ds_read_b64_tr_b16 a[140:141], v234 offset:34816\n\t"
    "ds_read_b64_tr_b16 a[142:143], v234 offset:35840\n\t"
    "ds_read_b64_tr_b16 a[148:149], v235 offset:34816\n\t"
    "v_mfma_f32_32x32x16_bf16 v[160:175], v[156:159], a[252:255], v[160:175]\n\t"
    "ds_read_b64_tr_b16 a[150:151], v235 offset:35840\n\t"
    "ds_read_b64_tr_b16 a[156:157], v236 offset:34816\n\t"
    "ds_read_b64_tr_b16 a[158:159], v236 offset:35840\n\t"
    "s_waitcnt lgkmcnt(14)\n\t"
    "v_mfma_f32_32x32x16_bf16 v[160:175], a[128:131], v[112:115], v[160:175]\n\t"
    "v_mov_b32 v208, s84\n\t"
    "v_mov_b32 v209, s85\n\t"
    "s_waitcnt lgkmcnt(12)\n\t"
    "v_mfma_f32_32x32x16_bf16 v[160:175], a[136:139], a[208:211], v[160:175]\n\t"
    "buffer_load_dwordx4 v247, s[36:39], s80 offen offset:1024 lds\n\t"
    "s_waitcnt lgkmcnt(10)\n\t"
    "v_mfma_f32_32x32x16_bf16 v[160:175], a[144:147], a[216:219], v[160:175]\n\t"
    "s_waitcnt lgkmcnt(8)\n\t"
    "v_mfma_f32_32x32x16_bf16 v[160:175], a[152:155], a[224:227], v[160:175]\n\t"
    "s_waitcnt lgkmcnt(6)\n\t"
    "v_mfma_f32_32x32x16_bf16 v[160:175], a[132:135], v[116:119], v[160:175]\n\t"
    "buffer_load_dwordx4 v248, s[36:39], s80 offen offset:2048 lds\n\t"
    "s_waitcnt lgkmcnt(4)\n\t"
    "v_mfma_f32_32x32x16_bf16 v[160:175], a[140:143], a[212:215], v[160:175]\n\t"
    "s_waitcnt lgkmcnt(2)\n\t"
    "v_mfma_f32_32x32x16_bf16 v[160:175], a[148:151], a[220:223], v[160:175]\n\t"
    "s_waitcnt lgkmcnt(0)\n\t"
    "v_mfma_f32_32x32x16_bf16 v[160:175], a[156:159], a[228:231], v[160:175]\n\t"
    "buffer_load_dwordx4 v249, s[36:39], s80 offen offset:3072 lds\n\t"
    "s_waitcnt vmcnt(12)\n\t"
    "s_barrier\n\t"
    "ds_read_b64_tr_b16 v[128:129], v244 offset:36864\n\t"
    "ds_read_b64_tr_b16 v[130:131], v244 offset:37888\n\t"
    "ds_read_b64_tr_b16 v[136:137], v234 offset:36864\n\t"
    "ds_read_b64_tr_b16 v[138:139], v234 offset:37888\n\t"
    "ds_read_b64_tr_b16 v[144:145], v235 offset:36864\n\t"
    "ds_read_b64_tr_b16 v[146:147], v235 offset:37888\n\t"
    "ds_read_b64_tr_b16 v[152:153], v236 offset:36864\n\t"
    "ds_read_b64_tr_b16 v[154:155], v236 offset:37888\n\t"
    "ds_read_b64_tr_b16 v[132:133], v244 offset:38912\n\t"
    "ds_read_b64_tr_b16 v[134:135], v244 offset:39936\n\t"
    "ds_read_b64_tr_b16 v[140:141], v234 offset:38912\n\t"
    "ds_read_b64_tr_b16 v[142:143], v234 offset:39936\n\t"
    "ds_read_b64_tr_b16 v[148:149], v235 offset:38912\n\t"
    "ds_read_b64_tr_b16 v[150:151], v235 offset:39936\n\t"
    "ds_read_b64_tr_b16 v[156:157], v236 offset:38912\n\t"
    "ds_read_b64_tr_b16 v[158:159], v236 offset:39936\n\t"
    "s_waitcnt lgkmcnt(14)\n\t"
    "v_mfma_f32_32x32x16_bf16 v[160:175], v[128:131], v[200:203], v[160:175]\n\t"
    "s_waitcnt lgkmcnt(12)\n\t"
    "v_mfma_f32_32x32x16_bf16 v[160:175], v[136:139], a[0:3], v[160:175]\n\t"
    "s_add_u32 m0, s56, 4096\n\t"
    "s_lshr_b32 s80, s59, 12\n\t"
    "s_add_u32 s80, s80, 28672\n\t"
    "buffer_load_dwordx4 v246, s[36:39], s80 offen lds\n\t"
    "s_waitcnt lgkmcnt(10)\n\t"
    "v_mfma_f32_32x32x16_bf16 v[160:175], v[144:147], a[8:11], v[160:175]\n\t"
    "s_waitcnt lgkmcnt(8)\n\t"
    "v_mfma_f32_32x32x16_bf16 v[160:175], v[152:155], a[16:19], v[160:175]\n\t"
    "buffer_load_dwordx4 v247, s[36:39], s80 offen offset:1024 lds\n\t"
    "s_waitcnt lgkmcnt(6)\n\t"
    "v_mfma_f32_32x32x16_bf16 v[160:175], v[132:135], v[204:207], v[160:175]\n\t"
    "s_waitcnt lgkmcnt(4)\n\t"
    "v_mfma_f32_32x32x16_bf16 v[160:175], v[140:143], a[4:7], v[160:175]\n\t"
    "buffer_load_dwordx4 v248, s[36:39], s80 offen offset:2048 lds\n\t"
    "s_waitcnt lgkmcnt(2)\n\t"
    "v_mfma_f32_32x32x16_bf16 v[160:175], v[148:151], a[12:15], v[160:175]\n\t"
    "s_waitcnt lgkmcnt(0)\n\t"
    "v_mfma_f32_32x32x16_bf16 v[160:175], v[156:159], a[20:23], v[160:175]\n\t"
    "buffer_load_dwordx4 v249, s[36:39], s80 offen offset:3072 lds\n\t"
    "s_nop 10\n\t"
    "v_mul_f32 v160, v160, v216\n\t"
    "v_mul_f32 v161, v161, v216\n\t"
    "v_mul_f32 v162, v162, v216\n\t"
    "v_mul_f32 v163, v163, v216\n\t"
    "v_mul_f32 v168, v168, v216\n\t"
    "v_mul_f32 v169, v169, v216\n\t"
    "v_mul_f32 v170, v170, v216\n\t"
    "v_mul_f32 v171, v171, v216\n\t"
    "s_add_u32 m0, s56, 24576\n\t"
    "s_lshr_b32 s80, s60, 12\n\t"
    "buffer_load_dwordx4 v246, s[36:39], s80 offen lds\n\t"
    "LT0_1_%=:\n\t"
    "s_cbranch_execz LTr_%=\n\t"
    "v_cvt_pk_bf16_f32 v160, v160, v161\n\t"
    "v_cvt_pk_bf16_f32 v161, v162, v163\n\t"
    "v_cvt_pk_bf16_f32 v162, v168, v169\n\t"
    "v_cvt_pk_bf16_f32 v163, v170, v171\n\t"
    "ds_write_b128 v237, v[160:163] offset:30720\n\t"
    "v_mul_f32 v164, v164, v216\n\t"
    "v_mul_f32 v165, v165, v216\n\t"
    "v_mul_f32 v166, v166, v216\n\t"
    "buffer_load_dwordx4 v247, s[36:39], s80 offen offset:1024 lds\n\t"
    "v_mul_f32 v167, v167, v216\n\t"
    "v_mul_f32 v172, v172, v216\n\t"
    "v_mul_f32 v173, v173, v216\n\t"
    "v_mul_f32 v174, v174, v216\n\t"
    "v_mul_f32 v175, v175, v216\n\t"
    "v_cvt_pk_bf16_f32 v164, v164, v165\n\t"
    "v_cvt_pk_bf16_f32 v165, v166, v167\n\t"
    "v_cvt_pk_bf16_f32 v166, v172, v173\n\t"
    "buffer_load_dwordx4 v248, s[36:39], s80 offen offset:2048 lds\n\t"
    "v_cvt_pk_bf16_f32 v167, v174, v175\n\t"
    "ds_write_b128 v237, v[164:167] offset:31232\n\t"
    "s_waitcnt lgkmcnt(0)\n\t"
    "s_barrier\n\t"
    "ds_read_b64_tr_b16 v[0:1], v250 offset:20480\n\t"
    "ds_read_b64_tr_b16 v[2:3], v250 offset:22528\n\t"
    "ds_read_b64_tr_b16 v[4:5], v250 offset:0\n\t"
    "ds_read_b64_tr_b16 v[6:7], v250 offset:2048\n\t"
    "buffer_load_dwordx4 v249, s[36:39], s80 offen offset:3072 lds\n\t"
    "ds_read_b128 a[160:163], v254 offset:2048\n\t"
    "ds_read_b128 a[164:167], v254 offset:3072\n\t"
    "ds_read_b128 a[168:171], v254 offset:43008\n\t"
    "ds_read_b128 a[172:175], v254 offset:44032\n\t"
    "s_add_u32 m0, s56, 32768\n\t"
    "s_lshr_b32 s80, s60, 12\n\t"
    "s_add_u32 s80, s80, 28672\n\t"
    "buffer_load_dwordx4 v246, s[36:39], s80 offen lds\n\t"
    "ds_read_b128 a[176:179], v255 offset:2048\n\t"
    "ds_read_b128 a[180:183], v255 offset:3072\n\t"
    "ds_read_b128 a[184:187], v255 offset:43008\n\t"
    "ds_read_b128 a[188:191], v255 offset:44032\n\t"
    "buffer_load_dwordx4 v247, s[36:39], s80 offen offset:1024 lds\n\t"
    "s_waitcnt lgkmcnt(10)\n\t"
    "v_mfma_f32_16x16x32_bf16 v[64:67], v[0:3], a[32:35], 0\n\t"
    "buffer_load_dwordx4 v248, s[36:39], s80 offen offset:2048 lds\n\t"
    "ds_read_b64_tr_b16 v[8:9], v226 offset:20480\n\t"
    "ds_read_b64_tr_b16 v[10:11], v226 offset:22528\n\t"
    "v_mfma_f32_16x16x32_bf16 v[68:71], v[0:3], a[36:39], 0\n\t"
    "buffer_load_dwordx4 v249, s[36:39], s80 offen offset:3072 lds\n\t"
    "ds_read_b64_tr_b16 v[12:13], v226 offset:0\n\t"
    "ds_read_b64_tr_b16 v[14:15], v226 offset:2048\n\t"
    "v_mfma_f32_16x16x32_bf16 v[72:75], v[0:3], a[40:43], 0\n\t"
    "s_add_u32 m0, s56, 36864\n\t"
    "s_lshr_b32 s80, s61, 12\n\t"
    "buffer_load_dwordx4 v246, s[36:39], s80 offen lds\n\t"
    "ds_read_b64_tr_b16 v[16:17], v227 offset:20480\n\t"
    "ds_read_b64_tr_b16 v[18:19], v227 offset:22528\n\t"
    "v_mfma_f32_16x16x32_bf16 v[76:79], v[0:3], a[44:47], 0\n\t"
    "buffer_load_dwordx4 v247, s[36:39], s80 offen offset:1024 lds\n\t"
    "ds_read_b64_tr_b16 v[20:21], v227 offset:0\n\t"
    "ds_read_b64_tr_b16 v[22:23], v227 offset:2048\n\t"
    "s_waitcnt lgkmcnt(14)\n\t"
    "v_mfma_f32_16x16x32_bf16 v[64:67], v[4:7], a[48:51], v[64:67]\n\t"
    "ds_read_b64_tr_b16 v[24:25], v228 offset:20480\n\t"
    "ds_read_b64_tr_b16 v[26:27], v228 offset:22528\n\t"
    "v_mfma_f32_16x16x32_bf16 v[68:71], v[4:7], a[52:55], v[68:71]\n\t"
    "buffer_load_dwordx4 v248, s[36:39], s80 offen offset:2048 lds\n\t"
    "ds_read_b64_tr_b16 v[28:29], v228 offset:0\n\t"
    "ds_read_b64_tr_b16 v[30:31], v228 offset:2048\n\t"
    "v_mfma_f32_16x16x32_bf16 v[72:75], v[4:7], a[56:59], v[72:75]\n\t"
    "v_mfma_f32_16x16x32_bf16 v[76:79], v[4:7], a[60:63], v[76:79]\n\t"
    "s_waitcnt lgkmcnt(10)\n\t"
    "v_mfma_f32_16x16x32_bf16 v[64:67], v[8:11], a[64:67], v[64:67]\n\t"
    "buffer_load_dwordx4 v249, s[36:39], s80 offen offset:3072 lds\n\t"
    "v_mfma_f32_16x16x32_bf16 v[68:71], v[8:11], a[68:71], v[68:71]\n\t"
    "v_mfma_f32_16x16x32_bf16 v[72:75], v[8:11], a[72:75], v[72:75]\n\t"
    "v_mfma_f32_16x16x32_bf16 v[76:79], v[8:11], a[76:79], v[76:79]\n\t"
    "s_waitcnt lgkmcnt(8)\n\t"
    "v_mfma_f32_16x16x32_bf16 v[64:67], v[12:15], a[80:83], v[64:67]\n\t"
    "s_waitcnt vmcnt(24)\n\t"
    "s_waitcnt vmcnt(20)\n\t"
    "s_waitcnt lgkmcnt(0)\n\t"
    "s_barrier\n\t"
    "ds_read_b64_tr_b16 v[32:33], v250 offset:8192\n\t"
    "v_mfma_f32_16x16x32_bf16 v[68:71], v[12:15], a[84:87], v[68:71]\n\t"
    "s_add_u32 m0, s56, 20480\n\t"
    "s_lshr_b32 s80, s61, 12\n\t"
    "s_add_u32 s80, s80, 28672\n\t"
    "buffer_load_dwordx4 v246, s[36:39], s80 offen lds\n\t"
    "ds_read_b64_tr_b16 v[34:35], v250 offset:10240\n\t"
    "v_mfma_f32_16x16x32_bf16 v[72:75], v[12:15], a[88:91], v[72:75]\n\t"
    "buffer_load_dwordx4 v247, s[36:39], s80 offen offset:1024 lds\n\t"
    "ds_read_b64_tr_b16 v[36:37], v250 offset:12288\n\t"
    "v_mfma_f32_16x16x32_bf16 v[76:79], v[12:15], a[92:95], v[76:79]\n\t"
    "ds_read_b64_tr_b16 v[38:39], v250 offset:14336\n\t"
    "v_mfma_f32_16x16x32_bf16 v[64:67], v[16:19], a[96:99], v[64:67]\n\t"
    "ds_read_b64_tr_b16 v[40:41], v226 offset:8192\n\t"
    "ds_read_b64_tr_b16 v[42:43], v226 offset:10240\n\t"
    "v_mfma_f32_16x16x32_bf16 v[68:71], v[16:19], a[100:103], v[68:71]\n\t"
    "buffer_load_dwordx4 v248, s[36:39], s80 offen offset:2048 lds\n\t"
    "ds_read_b64_tr_b16 v[44:45], v226 offset:12288\n\t"
    "v_mfma_f32_16x16x32_bf16 v[72:75], v[16:19], a[104:107], v[72:75]\n\t"
    "ds_read_b64_tr_b16 v[46:47], v226 offset:14336\n\t"
    "v_mfma_f32_16x16x32_bf16 v[76:79], v[16:19], a[108:111], v[76:79]\n\t"
    "ds_read_b64_tr_b16 v[48:49], v227 offset:8192\n\t"
    "v_mfma_f32_16x16x32_bf16 v[64:67], v[20:23], a[112:115], v[64:67]\n\t"
    "buffer_load_dwordx4 v249, s[36:39], s80 offen offset:3072 lds\n\t"
    "ds_read_b64_tr_b16 v[50:51], v227 offset:10240\n\t"
    "v_mfma_f32_16x16x32_bf16 v[68:71], v[20:23], a[116:119], v[68:71]\n\t"
    "ds_read_b64_tr_b16 v[52:53], v227 offset:12288\n\t"
    "ds_read_b64_tr_b16 v[54:55], v227 offset:14336\n\t"
    "v_mfma_f32_16x16x32_bf16 v[72:75], v[20:23], a[120:123], v[72:75]\n\t"
    "ds_read_b64_tr_b16 v[56:57], v228 offset:8192\n\t"
    "LT1_0_%=:\n\t"
    "s_cbranch_execz LT1_1_%=\n\t"
    "v_mfma_f32_16x16x32_bf16 v[76:79], v[20:23], a[124:127], v[76:79]\n\t"
    "s_add_u32 m0, s56, 0\n\t"
    "s_lshr_b32 s80, s62, 12\n\t"
    "buffer_load_dwordx4 v246, s[36:39], s80 offen lds\n\t"
    "ds_read_b64_tr_b16 v[58:59], v228 offset:10240\n\t"
    "v_mfma_f32_16x16x32_bf16 v[64:67], v[24:27], a[160:163], v[64:67]\n\t"
    "ds_read_b64_tr_b16 v[60:61], v228 offset:12288\n\t"
    "v_mfma_f32_16x16x32_bf16 v[68:71], v[24:27], a[164:167], v[68:71]\n\t"
    "ds_read_b64_tr_b16 v[62:63], v228 offset:14336\n\t"
    "v_mfma_f32_16x16x32_bf16 v[72:75], v[24:27], a[168:171], v[72:75]\n\t"
    "buffer_load_dwordx4 v247, s[36:39], s80 offen offset:1024 lds\n\t"
    "v_mfma_f32_16x16x32_bf16 v[76:79], v[24:27], a[172:175], v[76:79]\n\t"
    "v_mfma_f32_16x16x32_bf16 v[64:67], v[28:31], a[176:179], v[64:67]\n\t"
    "v_mfma_f32_16x16x32_bf16 v[68:71], v[28:31], a[180:183], v[68:71]\n\t"
    "v_mfma_f32_16x16x32_bf16 v[72:75], v[28:31], a[184:187], v[72:75]\n\t"
    "buffer_load_dwordx4 v248, s[36:39], s80 offen offset:2048 lds\n\t"
    "v_mfma_f32_16x16x32_bf16 v[76:79], v[28:31], a[188:191], v[76:79]\n\t"
    "s_waitcnt lgkmcnt(14)\n\t"
    "v_mfma_f32_16x16x32_bf16 v[80:83], v[32:35], a[32:35], 0\n\t"
    "v_mfma_f32_16x16x32_bf16 v[84:87], v[32:35], a[36:39], 0\n\t"
    "s_waitcnt vmcnt(23)\n\t"
    "s_waitcnt vmcnt(19)\n\t"
    "s_waitcnt lgkmcnt(0)\n\t"
    "s_barrier\n\t"
    "v_mfma_f32_16x16x32_bf16 v[88:91], v[32:35], a[40:43], 0\n\t"
    "buffer_load_dwordx4 v249, s[36:39], s80 offen offset:3072 lds\n\t"
    "v_cvt_pk_bf16_f32 v96, v64, v65\n\t"
    "ds_read_b64_tr_b16 v[0:1], v250 offset:16384\n\t"
    "v_mfma_f32_16x16x32_bf16 v[92:95], v[32:35], a[44:47], 0\n\t"
    "s_add_u32 m0, s56, 28672\n\t"
    "s_lshr_b32 s80, s62, 12\n\t"
    "s_add_u32 s80, s80, 28672\n\t"
    "buffer_load_dwordx4 v246, s[36:39], s80 offen lds\n\t"
    "v_cvt_pk_bf16_f32 v97, v66, v67\n\t"
    "ds_read_b64_tr_b16 v[2:3], v250 offset:18432\n\t"
    "v_mfma_f32_16x16x32_bf16 v[80:83], v[36:39], a[48:51], v[80:83]\n\t"
    "v_cvt_pk_bf16_f32 v98, v68, v69\n\t"
    "v_mfma_f32_16x16x32_bf16 v[84:87], v[36:39], a[52:55], v[84:87]\n\t"
    "buffer_load_dwordx4 v247, s[36:39], s80 offen offset:1024 lds\n\t"
    "v_cvt_pk_bf16_f32 v99, v70, v71\n\t"
    "ds_read_b64_tr_b16 v[4:5], v250 offset:4096\n\t"
    "v_mfma_f32_16x16x32_bf16 v[88:91], v[36:39], a[56:59], v[88:91]\n\t"
    "s_mov_b32 s74, 0\n\t"
    "buffer_store_dwordx4 v[96:99], v225, s[40:43], s74 offen sc1\n\t"
    "ds_read_b64_tr_b16 v[6:7], v250 offset:6144\n\t"
    "v_mfma_f32_16x16x32_bf16 v[92:95], v[36:39], a[60:63], v[92:95]\n\t"
    "v_cvt_pk_bf16_f32 v100, v72, v73\n\t"
    "v_mfma_f32_16x16x32_bf16 v[80:83], v[40:43], a[64:67], v[80:83]\n\t"
    "v_cvt_pk_bf16_f32 v101, v74, v75\n\t"
    "ds_read_b64_tr_b16 v[8:9], v226 offset:16384\n\t"
    "v_mfma_f32_16x16x32_bf16 v[84:87], v[40:43], a[68:71], v[84:87]\n\t"
    "v_cvt_pk_bf16_f32 v102, v76, v77\n\t"
    "ds_read_b64_tr_b16 v[10:11], v226 offset:18432\n\t"
    "v_mfma_f32_16x16x32_bf16 v[88:91], v[40:43], a[72:75], v[88:91]\n\t"
    "v_cvt_pk_bf16_f32 v103, v78, v79\n\t"
    "v_mfma_f32_16x16x32_bf16 v[92:95], v[40:43], a[76:79], v[92:95]\n\t"
    "buffer_load_dwordx4 v248, s[36:39], s80 offen offset:2048 lds\n\t"
    "s_mov_b32 s74, 1024\n\t"
    "buffer_store_dwordx4 v[100:103], v225, s[40:43], s74 offen sc1\n\t"
    "ds_read_b64_tr_b16 v[12:13], v226 offset:4096\n\t"
    "v_mfma_f32_16x16x32_bf16 v[80:83], v[44:47], a[80:83], v[80:83]\n\t"
    "ds_read_b64_tr_b16 v[14:15], v226 offset:6144\n\t"
    "v_mfma_f32_16x16x32_bf16 v[84:87], v[44:47], a[84:87], v[84:87]\n\t"
    "v_mfma_f32_16x16x32_bf16 v[88:91], v[44:47], a[88:91], v[88:91]\n\t"
    "ds_read_b64_tr_b16 v[16:17], v227 offset:16384\n\t"
    "v_mfma_f32_16x16x32_bf16 v[92:95], v[44:47], a[92:95], v[92:95]\n\t"
    "ds_read_b64_tr_b16 v[18:19], v227 offset:18432\n\t"
    "v_mfma_f32_16x16x32_bf16 v[80:83], v[48:51], a[96:99], v[80:83]\n\t"
    "v_mfma_f32_16x16x32_bf16 v[84:87], v[48:51], a[100:103], v[84:87]\n\t"
    "ds_read_b64_tr_b16 v[20:21], v227 offset:4096\n\t"
    "v_mfma_f32_16x16x32_bf16 v[88:91], v[48:51], a[104:107], v[88:91]\n\t"
    "buffer_load_dwordx4 v249, s[36:39], s80 offen offset:3072 lds\n\t"
    "ds_read_b64_tr_b16 v[22:23], v227 offset:6144\n\t"
    "v_mfma_f32_16x16x32_bf16 v[92:95], v[48:51], a[108:111], v[92:95]\n\t"
    "v_mfma_f32_16x16x32_bf16 v[80:83], v[52:55], a[112:115], v[80:83]\n\t"
    "ds_read_b64_tr_b16 v[24:25], v228 offset:16384\n\t"
    "v_mfma_f32_16x16x32_bf16 v[84:87], v[52:55], a[116:119], v[84:87]\n\t"
    "s_add_u32 m0, s56, 8192\n\t"
    "s_lshr_b32 s80, s63, 12\n\t"
    "buffer_load_dwordx4 v246, s[36:39], s80 offen lds\n\t"
    "ds_read_b64_tr_b16 v[26:27], v228 offset:18432\n\t"
    "v_mfma_f32_16x16x32_bf16 v[88:91], v[52:55], a[120:123], v[88:91]\n\t"
    "v_mfma_f32_16x16x32_bf16 v[92:95], v[52:55], a[124:127], v[92:95]\n\t"
    "ds_read_b64_tr_b16 v[28:29], v228 offset:4096\n\t"
    "v_mfma_f32_16x16x32_bf16 v[80:83], v[56:59], a[160:163], v[80:83]\n\t"
    "buffer_load_dwordx4 v247, s[36:39], s80 offen offset:1024 lds\n\t"
    "ds_read_b64_tr_b16 v[30:31], v228 offset:6144\n\t"
    "v_mfma_f32_16x16x32_bf16 v[84:87], v[56:59], a[164:167], v[84:87]\n\t"
    "v_mfma_f32_16x16x32_bf16 v[88:91], v[56:59], a[168:171], v[88:91]\n\t"
    "v_mfma_f32_16x16x32_bf16 v[92:95], v[56:59], a[172:175], v[92:95]\n\t"
    "v_mfma_f32_16x16x32_bf16 v[80:83], v[60:63], a[176:179], v[80:83]\n\t"
    "buffer_load_dwordx4 v248, s[36:39], s80 offen offset:2048 lds\n\t"
    "v_mfma_f32_16x16x32_bf16 v[84:87], v[60:63], a[180:183], v[84:87]\n\t"
    "v_mfma_f32_16x16x32_bf16 v[88:91], v[60:63], a[184:187], v[88:91]\n\t"
    "v_mfma_f32_16x16x32_bf16 v[92:95], v[60:63], a[188:191], v[92:95]\n\t"
    "s_waitcnt lgkmcnt(14)\n\t"
    "v_mfma_f32_16x16x32_bf16 v[64:67], v[0:3], a[32:35], 0\n\t"
    "buffer_load_dwordx4 v249, s[36:39], s80 offen offset:3072 lds\n\t"
    "v_mfma_f32_16x16x32_bf16 v[68:71], v[0:3], a[36:39], 0\n\t"
    "s_waitcnt vmcnt(26)\n\t"
    "s_waitcnt vmcnt(22)\n\t"
    "s_waitcnt lgkmcnt(0)\n\t"
    "s_barrier\n\t"
    "v_mfma_f32_16x16x32_bf16 v[72:75], v[0:3], a[40:43], 0\n\t"
    "s_add_u32 m0, s56, 12288\n\t"
    "s_lshr_b32 s80, s63, 12\n\t"
    "s_add_u32 s80, s80, 28672\n\t"
    "buffer_load_dwordx4 v246, s[36:39], s80 offen lds\n\t"
    "v_cvt_pk_bf16_f32 v104, v80, v81\n\t"
    "ds_read_b64_tr_b16 v[32:33], v250 offset:24576\n\t"
    "v_mfma_f32_16x16x32_bf16 v[76:79], v[0:3], a[44:47], 0\n\t"
    "LT1_1_%=:\n\t"
    "s_cbranch_execz LTr_%=\n\t"
    "v_cvt_pk_bf16_f32 v105, v82, v83\n\t"
    "ds_read_b64_tr_b16 v[34:35], v250 offset:26624\n\t"
    "v_mfma_f32_16x16x32_bf16 v[64:67], v[4:7], a[48:51], v[64:67]\n\t"
    "buffer_load_dwordx4 v247, s[36:39], s80 offen offset:1024 lds\n\t"
    "v_cvt_pk_bf16_f32 v106, v84, v85\n\t"
    "v_mfma_f32_16x16x32_bf16 v[68:71], v[4:7], a[52:55], v[68:71]\n\t"
    "buffer_load_dwordx4 v248, s[36:39], s80 offen offset:2048 lds\n\t"
    "v_cvt_pk_bf16_f32 v107, v86, v87\n\t"
    "ds_read_b64_tr_b16 v[36:37], v250 offset:32768\n\t"
    "v_mfma_f32_16x16x32_bf16 v[72:75], v[4:7], a[56:59], v[72:75]\n\t"
    "s_mov_b32 s74, 8192\n\t"
    "buffer_store_dwordx4 v[104:107], v225, s[40:43], s74 offen sc1\n\t"
    "ds_read_b64_tr_b16 v[38:39], v250 offset:34816\n\t"
    "v_mfma_f32_16x16x32_bf16 v[76:79], v[4:7], a[60:63], v[76:79]\n\t"
    "v_cvt_pk_bf16_f32 v108, v88, v89\n\t"
    "v_mfma_f32_16x16x32_bf16 v[64:67], v[8:11], a[64:67], v[64:67]\n\t"
    "v_cvt_pk_bf16_f32 v109, v90, v91\n\t"
    "ds_read_b64_tr_b16 v[40:41], v226 offset:24576\n\t"
    "v_mfma_f32_16x16x32_bf16 v[68:71], v[8:11], a[68:71], v[68:71]\n\t"
    "buffer_load_dwordx4 v249, s[36:39], s80 offen offset:3072 lds\n\t"
    "v_cvt_pk_bf16_f32 v110, v92, v93\n\t"
    "ds_read_b64_tr_b16 v[42:43], v226 offset:26624\n\t"
    "v_mfma_f32_16x16x32_bf16 v[72:75], v[8:11], a[72:75], v[72:75]\n\t"
    "v_cvt_pk_bf16_f32 v111, v94, v95\n\t"
    "v_mfma_f32_16x16x32_bf16 v[76:79], v[8:11], a[76:79], v[76:79]\n\t"
    "s_mov_b32 s74, 9216\n\t"
    "buffer_store_dwordx4 v[108:111], v225, s[40:43], s74 offen sc1\n\t"
    "ds_read_b64_tr_b16 v[44:45], v226 offset:32768\n\t"
    "v_mfma_f32_16x16x32_bf16 v[64:67], v[12:15], a[80:83], v[64:67]\n\t"
    "ds_read_b64_tr_b16 v[46:47], v226 offset:34816\n\t"
    "v_mfma_f32_16x16x32_bf16 v[68:71], v[12:15], a[84:87], v[68:71]\n\t"
    "v_mfma_f32_16x16x32_bf16 v[72:75], v[12:15], a[88:91], v[72:75]\n\t"
    "ds_read_b64_tr_b16 v[48:49], v227 offset:24576\n\t"
    "v_mfma_f32_16x16x32_bf16 v[76:79], v[12:15], a[92:95], v[76:79]\n\t"
    "ds_read_b64_tr_b16 v[50:51], v227 offset:26624\n\t"
    "v_mfma_f32_16x16x32_bf16 v[64:67], v[16:19], a[96:99], v[64:67]\n\t"
    "v_mfma_f32_16x16x32_bf16 v[68:71], v[16:19], a[100:103], v[68:71]\n\t"
    "ds_read_b64_tr_b16 v[52:53], v227 offset:32768\n\t"
    "v_mfma_f32_16x16x32_bf16 v[72:75], v[16:19], a[104:107], v[72:75]\n\t"
    "ds_read_b64_tr_b16 v[54:55], v227 offset:34816\n\t"
    "v_mfma_f32_16x16x32_bf16 v[76:79], v[16:19], a[108:111], v[76:79]\n\t"
    "v_mfma_f32_16x16x32_bf16 v[64:67], v[20:23], a[112:115], v[64:67]\n\t"
    "ds_read_b64_tr_b16 v[56:57], v228 offset:24576\n\t"
    "v_mfma_f32_16x16x32_bf16 v[68:71], v[20:23], a[116:119], v[68:71]\n\t"
    "ds_read_b64_tr_b16 v[58:59], v228 offset:26624\n\t"
    "v_mfma_f32_16x16x32_bf16 v[72:75], v[20:23], a[120:123], v[72:75]\n\t"
    "v_mfma_f32_16x16x32_bf16 v[76:79], v[20:23], a[124:127], v[76:79]\n\t"
    "ds_read_b64_tr_b16 v[60:61], v228 offset:32768\n\t"
    "v_mfma_f32_16x16x32_bf16 v[64:67], v[24:27], a[160:163], v[64:67]\n\t"
    "ds_read_b64_tr_b16 v[62:63], v228 offset:34816\n\t"
    "v_mfma_f32_16x16x32_bf16 v[68:71], v[24:27], a[164:167], v[68:71]\n\t"
    "v_mfma_f32_16x16x32_bf16 v[72:75], v[24:27], a[168:171], v[72:75]\n\t"
    "v_mfma_f32_16x16x32_bf16 v[76:79], v[24:27], a[172:175], v[76:79]\n\t"
    "v_mfma_f32_16x16x32_bf16 v[64:67], v[28:31], a[176:179], v[64:67]\n\t"
    "v_mfma_f32_16x16x32_bf16 v[68:71], v[28:31], a[180:183], v[68:71]\n\t"
    "v_mfma_f32_16x16x32_bf16 v[72:75], v[28:31], a[184:187], v[72:75]\n\t"
    "v_mfma_f32_16x16x32_bf16 v[76:79], v[28:31], a[188:191], v[76:79]\n\t"
    "s_waitcnt lgkmcnt(14)\n\t"
    "v_mfma_f32_16x16x32_bf16 v[80:83], v[32:35], a[32:35], 0\n\t"
    "v_mfma_f32_16x16x32_bf16 v[84:87], v[32:35], a[36:39], 0\n\t"
    "s_waitcnt vmcnt(24)\n\t"
    "s_waitcnt vmcnt(20)\n\t"
    "s_waitcnt lgkmcnt(0)\n\t"
    "s_barrier\n\t"
    "v_mfma_f32_16x16x32_bf16 v[88:91], v[32:35], a[40:43], 0\n\t"
    "v_cvt_pk_bf16_f32 v112, v64, v65\n\t"
    "ds_read_b64_tr_b16 v[0:1], v250 offset:36864\n\t"
    "v_mfma_f32_16x16x32_bf16 v[92:95], v[32:35], a[44:47], 0\n\t"
    "v_cvt_pk_bf16_f32 v113, v66, v67\n\t"
    "ds_read_b64_tr_b16 v[2:3], v250 offset:38912\n\t"
    "v_mfma_f32_16x16x32_bf16 v[80:83], v[36:39], a[48:51], v[80:83]\n\t"
    "v_cvt_pk_bf16_f32 v114, v68, v69\n\t"
    "v_mfma_f32_16x16x32_bf16 v[84:87], v[36:39], a[52:55], v[84:87]\n\t"
    "v_cvt_pk_bf16_f32 v115, v70, v71\n\t"
    "ds_read_b64_tr_b16 v[4:5], v250 offset:20480\n\t"
    "v_mfma_f32_16x16x32_bf16 v[88:91], v[36:39], a[56:59], v[88:91]\n\t"
    "s_mov_b32 s74, 16384\n\t"
    "buffer_store_dwordx4 v[112:115], v225, s[40:43], s74 offen sc1\n\t"
    "ds_read_b64_tr_b16 v[6:7], v250 offset:22528\n\t"
    "v_mfma_f32_16x16x32_bf16 v[92:95], v[36:39], a[60:63], v[92:95]\n\t"
    "v_cvt_pk_bf16_f32 v116, v72, v73\n\t"
    "v_mfma_f32_16x16x32_bf16 v[80:83], v[40:43], a[64:67], v[80:83]\n\t"
    "v_cvt_pk_bf16_f32 v117, v74, v75\n\t"
    "ds_read_b64_tr_b16 v[8:9], v226 offset:36864\n\t"
    "v_mfma_f32_16x16x32_bf16 v[84:87], v[40:43], a[68:71], v[84:87]\n\t"
    "v_cvt_pk_bf16_f32 v118, v76, v77\n\t"
    "ds_read_b64_tr_b16 v[10:11], v226 offset:38912\n\t"
    "v_mfma_f32_16x16x32_bf16 v[88:91], v[40:43], a[72:75], v[88:91]\n\t"
    "v_cvt_pk_bf16_f32 v119, v78, v79\n\t"
    "v_mfma_f32_16x16x32_bf16 v[92:95], v[40:43], a[76:79], v[92:95]\n\t"
    "s_mov_b32 s74, 17408\n\t"
    "buffer_store_dwordx4 v[116:119], v225, s[40:43], s74 offen sc1\n\t"
    "ds_read_b64_tr_b16 v[12:13], v226 offset:20480\n\t"
    "v_mfma_f32_16x16x32_bf16 v[80:83], v[44:47], a[80:83], v[80:83]\n\t"
    "ds_read_b64_tr_b16 v[14:15], v226 offset:22528\n\t"
    "v_mfma_f32_16x16x32_bf16 v[84:87], v[44:47], a[84:87], v[84:87]\n\t"
    "v_mfma_f32_16x16x32_bf16 v[88:91], v[44:47], a[88:91], v[88:91]\n\t"
    "ds_read_b64_tr_b16 v[16:17], v227 offset:36864\n\t"
    "v_mfma_f32_16x16x32_bf16 v[92:95], v[44:47], a[92:95], v[92:95]\n\t"
    "ds_read_b64_tr_b16 v[18:19], v227 offset:38912\n\t"
    "v_mfma_f32_16x16x32_bf16 v[80:83], v[48:51], a[96:99], v[80:83]\n\t"
    "v_mfma_f32_16x16x32_bf16 v[84:87], v[48:51], a[100:103], v[84:87]\n\t"
    "ds_read_b64_tr_b16 v[20:21], v227 offset:20480\n\t"
    "v_mfma_f32_16x16x32_bf16 v[88:91], v[48:51], a[104:107], v[88:91]\n\t"
    "ds_read_b64_tr_b16 v[22:23], v227 offset:22528\n\t"
    "v_mfma_f32_16x16x32_bf16 v[92:95], v[48:51], a[108:111], v[92:95]\n\t"
    "v_mfma_f32_16x16x32_bf16 v[80:83], v[52:55], a[112:115], v[80:83]\n\t"
    "ds_read_b64_tr_b16 v[24:25], v228 offset:36864\n\t"
    "v_mfma_f32_16x16x32_bf16 v[84:87], v[52:55], a[116:119], v[84:87]\n\t"
    "ds_read_b64_tr_b16 v[26:27], v228 offset:38912\n\t"
    "v_mfma_f32_16x16x32_bf16 v[88:91], v[52:55], a[120:123], v[88:91]\n\t"
    "LT2_0_%=:\n\t"
    "s_cbranch_execz LT2_1_%=\n\t"
    "v_mfma_f32_16x16x32_bf16 v[92:95], v[52:55], a[124:127], v[92:95]\n\t"
    "ds_read_b64_tr_b16 v[28:29], v228 offset:20480\n\t"
    "v_mfma_f32_16x16x32_bf16 v[80:83], v[56:59], a[160:163], v[80:83]\n\t"
    "ds_read_b64_tr_b16 v[30:31], v228 offset:22528\n\t"
    "v_mfma_f32_16x16x32_bf16 v[84:87], v[56:59], a[164:167], v[84:87]\n\t"
    "v_mfma_f32_16x16x32_bf16 v[88:91], v[56:59], a[168:171], v[88:91]\n\t"
    "v_mfma_f32_16x16x32_bf16 v[92:95], v[56:59], a[172:175], v[92:95]\n\t"
    "v_mfma_f32_16x16x32_bf16 v[80:83], v[60:63], a[176:179], v[80:83]\n\t"
    "v_mfma_f32_16x16x32_bf16 v[84:87], v[60:63], a[180:183], v[84:87]\n\t"
    "v_mfma_f32_16x16x32_bf16 v[88:91], v[60:63], a[184:187], v[88:91]\n\t"
    "v_mfma_f32_16x16x32_bf16 v[92:95], v[60:63], a[188:191], v[92:95]\n\t"
    "s_waitcnt lgkmcnt(14)\n\t"
    "v_mfma_f32_16x16x32_bf16 v[64:67], v[0:3], a[32:35], 0\n\t"
    "v_mfma_f32_16x16x32_bf16 v[68:71], v[0:3], a[36:39], 0\n\t"
    "s_waitcnt vmcnt(18)\n\t"
    "s_waitcnt vmcnt(12)\n\t"
    "s_waitcnt lgkmcnt(0)\n\t"
    "s_barrier\n\t"
    "v_mfma_f32_16x16x32_bf16 v[72:75], v[0:3], a[40:43], 0\n\t"
    "v_cvt_pk_bf16_f32 v120, v80, v81\n\t"
    "ds_read_b64_tr_b16 v[32:33], v250 offset:0\n\t"
    "v_mfma_f32_16x16x32_bf16 v[76:79], v[0:3], a[44:47], 0\n\t"
    "v_cvt_pk_bf16_f32 v121, v82, v83\n\t"
    "ds_read_b64_tr_b16 v[34:35], v250 offset:2048\n\t"
    "v_mfma_f32_16x16x32_bf16 v[64:67], v[4:7], a[48:51], v[64:67]\n\t"
    "v_cvt_pk_bf16_f32 v122, v84, v85\n\t"
    "v_mfma_f32_16x16x32_bf16 v[68:71], v[4:7], a[52:55], v[68:71]\n\t"
    "v_cvt_pk_bf16_f32 v123, v86, v87\n\t"
    "ds_read_b64_tr_b16 v[36:37], v250 offset:28672\n\t"
    "v_mfma_f32_16x16x32_bf16 v[72:75], v[4:7], a[56:59], v[72:75]\n\t"
    "s_mov_b32 s74, 24576\n\t"
    "buffer_store_dwordx4 v[120:123], v225, s[40:43], s74 offen sc1\n\t"
    "ds_read_b64_tr_b16 v[38:39], v250 offset:30720\n\t"
    "v_mfma_f32_16x16x32_bf16 v[76:79], v[4:7], a[60:63], v[76:79]\n\t"
    "v_cvt_pk_bf16_f32 v124, v88, v89\n\t"
    "v_mfma_f32_16x16x32_bf16 v[64:67], v[8:11], a[64:67], v[64:67]\n\t"
    "v_cvt_pk_bf16_f32 v125, v90, v91\n\t"
    "ds_read_b64_tr_b16 v[40:41], v226 offset:0\n\t"
    "v_mfma_f32_16x16x32_bf16 v[68:71], v[8:11], a[68:71], v[68:71]\n\t"
    "v_cvt_pk_bf16_f32 v126, v92, v93\n\t"
    "ds_read_b64_tr_b16 v[42:43], v226 offset:2048\n\t"
    "v_mfma_f32_16x16x32_bf16 v[72:75], v[8:11], a[72:75], v[72:75]\n\t"
    "v_cvt_pk_bf16_f32 v127, v94, v95\n\t"
    "v_mfma_f32_16x16x32_bf16 v[76:79], v[8:11], a[76:79], v[76:79]\n\t"
    "s_mov_b32 s74, 25600\n\t"
    "buffer_store_dwordx4 v[124:127], v225, s[40:43], s74 offen sc1\n\t"
    "ds_read_b64_tr_b16 v[44:45], v226 offset:28672\n\t"
    "v_mfma_f32_16x16x32_bf16 v[64:67], v[12:15], a[80:83], v[64:67]\n\t"
    "ds_read_b64_tr_b16 v[46:47], v226 offset:30720\n\t"
    "v_mfma_f32_16x16x32_bf16 v[68:71], v[12:15], a[84:87], v[68:71]\n\t"
    "v_mfma_f32_16x16x32_bf16 v[72:75], v[12:15], a[88:91], v[72:75]\n\t"
    "ds_read_b64_tr_b16 v[48:49], v227 offset:0\n\t"
    "v_mfma_f32_16x16x32_bf16 v[76:79], v[12:15], a[92:95], v[76:79]\n\t"
    "ds_read_b64_tr_b16 v[50:51], v227 offset:2048\n\t"
    "v_mfma_f32_16x16x32_bf16 v[64:67], v[16:19], a[96:99], v[64:67]\n\t"
    "v_mfma_f32_16x16x32_bf16 v[68:71], v[16:19], a[100:103], v[68:71]\n\t"
    "ds_read_b64_tr_b16 v[52:53], v227 offset:28672\n\t"
    "v_mfma_f32_16x16x32_bf16 v[72:75], v[16:19], a[104:107], v[72:75]\n\t"
    "ds_read_b64_tr_b16 v[54:55], v227 offset:30720\n\t"
    "v_mfma_f32_16x16x32_bf16 v[76:79], v[16:19], a[108:111], v[76:79]\n\t"
    "v_mfma_f32_16x16x32_bf16 v[64:67], v[20:23], a[112:115], v[64:67]\n\t"
    "ds_read_b64_tr_b16 v[56:57], v228 offset:0\n\t"
    "v_mfma_f32_16x16x32_bf16 v[68:71], v[20:23], a[116:119], v[68:71]\n\t"
    "ds_read_b64_tr_b16 v[58:59], v228 offset:2048\n\t"
    "v_mfma_f32_16x16x32_bf16 v[72:75], v[20:23], a[120:123], v[72:75]\n\t"
    "v_mfma_f32_16x16x32_bf16 v[76:79], v[20:23], a[124:127], v[76:79]\n\t"
    "ds_read_b64_tr_b16 v[60:61], v228 offset:28672\n\t"
    "v_mfma_f32_16x16x32_bf16 v[64:67], v[24:27], a[160:163], v[64:67]\n\t"
    "ds_read_b64_tr_b16 v[62:63], v228 offset:30720\n\t"
    "v_mfma_f32_16x16x32_bf16 v[68:71], v[24:27], a[164:167], v[68:71]\n\t"
    "v_mfma_f32_16x16x32_bf16 v[72:75], v[24:27], a[168:171], v[72:75]\n\t"
    "v_mfma_f32_16x16x32_bf16 v[76:79], v[24:27], a[172:175], v[76:79]\n\t"
    "v_mfma_f32_16x16x32_bf16 v[64:67], v[28:31], a[176:179], v[64:67]\n\t"
    "v_mfma_f32_16x16x32_bf16 v[68:71], v[28:31], a[180:183], v[68:71]\n\t"
    "v_mfma_f32_16x16x32_bf16 v[72:75], v[28:31], a[184:187], v[72:75]\n\t"
    "v_mfma_f32_16x16x32_bf16 v[76:79], v[28:31], a[188:191], v[76:79]\n\t"
    "s_waitcnt lgkmcnt(14)\n\t"
    "v_mfma_f32_16x16x32_bf16 v[80:83], v[32:35], a[32:35], 0\n\t"
    "v_mfma_f32_16x16x32_bf16 v[84:87], v[32:35], a[36:39], 0\n\t"
    "s_waitcnt vmcnt(10)\n\t"
    "s_waitcnt vmcnt(5)\n\t"
    "s_waitcnt vmcnt(2)\n\t"
    "s_waitcnt lgkmcnt(0)\n\t"
    "s_barrier\n\t"
    "buffer_store_dwordx2 v[208:209], v229, s[44:47], 0 offen sc1\n\t"
    "v_mfma_f32_16x16x32_bf16 v[88:91], v[32:35], a[40:43], 0\n\t"
    "v_cvt_pk_bf16_f32 v128, v64, v65\n\t"
    "ds_read_b64_tr_b16 v[0:1], v250 offset:8192\n\t"
    "v_mfma_f32_16x16x32_bf16 v[92:95], v[32:35], a[44:47], 0\n\t"
    "v_cvt_pk_bf16_f32 v129, v66, v67\n\t"
    "ds_read_b64_tr_b16 v[2:3], v250 offset:10240\n\t"
    "v_mfma_f32_16x16x32_bf16 v[80:83], v[36:39], a[48:51], v[80:83]\n\t"
    "v_cvt_pk_bf16_f32 v130, v68, v69\n\t"
    "v_mfma_f32_16x16x32_bf16 v[84:87], v[36:39], a[52:55], v[84:87]\n\t"
    "v_cvt_pk_bf16_f32 v131, v70, v71\n\t"
    "ds_read_b64_tr_b16 v[4:5], v250 offset:12288\n\t"
    "v_mfma_f32_16x16x32_bf16 v[88:91], v[36:39], a[56:59], v[88:91]\n\t"
    "s_mov_b32 s74, 32768\n\t"
    "buffer_store_dwordx4 v[128:131], v225, s[40:43], s74 offen sc1\n\t"
    "ds_read_b64_tr_b16 v[6:7], v250 offset:14336\n\t"
    "v_mfma_f32_16x16x32_bf16 v[92:95], v[36:39], a[60:63], v[92:95]\n\t"
    "v_cvt_pk_bf16_f32 v132, v72, v73\n\t"
    "v_mfma_f32_16x16x32_bf16 v[80:83], v[40:43], a[64:67], v[80:83]\n\t"
    "v_cvt_pk_bf16_f32 v133, v74, v75\n\t"
    "ds_read_b64_tr_b16 v[8:9], v226 offset:8192\n\t"
    "v_mfma_f32_16x16x32_bf16 v[84:87], v[40:43], a[68:71], v[84:87]\n\t"
    "v_cvt_pk_bf16_f32 v134, v76, v77\n\t"
    "ds_read_b64_tr_b16 v[10:11], v226 offset:10240\n\t"
    "v_mfma_f32_16x16x32_bf16 v[88:91], v[40:43], a[72:75], v[88:91]\n\t"
    "v_cvt_pk_bf16_f32 v135, v78, v79\n\t"
    "v_mfma_f32_16x16x32_bf16 v[92:95], v[40:43], a[76:79], v[92:95]\n\t"
    "LT2_1_%=:\n\t"
    "s_cbranch_execz LTr_%=\n\t"
    "s_mov_b32 s74, 33792\n\t"
    "buffer_store_dwordx4 v[132:135], v225, s[40:43], s74 offen sc1\n\t"
    "ds_read_b64_tr_b16 v[12:13], v226 offset:12288\n\t"
    "v_mfma_f32_16x16x32_bf16 v[80:83], v[44:47], a[80:83], v[80:83]\n\t"
    "ds_read_b64_tr_b16 v[14:15], v226 offset:14336\n\t"
    "v_mfma_f32_16x16x32_bf16 v[84:87], v[44:47], a[84:87], v[84:87]\n\t"
    "v_mfma_f32_16x16x32_bf16 v[88:91], v[44:47], a[88:91], v[88:91]\n\t"
    "ds_read_b64_tr_b16 v[16:17], v227 offset:8192\n\t"
    "v_mfma_f32_16x16x32_bf16 v[92:95], v[44:47], a[92:95], v[92:95]\n\t"
    "ds_read_b64_tr_b16 v[18:19], v227 offset:10240\n\t"
    "v_mfma_f32_16x16x32_bf16 v[80:83], v[48:51], a[96:99], v[80:83]\n\t"
    "v_mfma_f32_16x16x32_bf16 v[84:87], v[48:51], a[100:103], v[84:87]\n\t"
    "ds_read_b64_tr_b16 v[20:21], v227 offset:12288\n\t"
    "v_mfma_f32_16x16x32_bf16 v[88:91], v[48:51], a[104:107], v[88:91]\n\t"
    "ds_read_b64_tr_b16 v[22:23], v227 offset:14336\n\t"
    "v_mfma_f32_16x16x32_bf16 v[92:95], v[48:51], a[108:111], v[92:95]\n\t"
    "v_mfma_f32_16x16x32_bf16 v[80:83], v[52:55], a[112:115], v[80:83]\n\t"
    "ds_read_b64_tr_b16 v[24:25], v228 offset:8192\n\t"
    "v_mfma_f32_16x16x32_bf16 v[84:87], v[52:55], a[116:119], v[84:87]\n\t"
    "ds_read_b64_tr_b16 v[26:27], v228 offset:10240\n\t"
    "v_mfma_f32_16x16x32_bf16 v[88:91], v[52:55], a[120:123], v[88:91]\n\t"
    "v_mfma_f32_16x16x32_bf16 v[92:95], v[52:55], a[124:127], v[92:95]\n\t"
    "ds_read_b64_tr_b16 v[28:29], v228 offset:12288\n\t"
    "v_mfma_f32_16x16x32_bf16 v[80:83], v[56:59], a[160:163], v[80:83]\n\t"
    "ds_read_b64_tr_b16 v[30:31], v228 offset:14336\n\t"
    "v_mfma_f32_16x16x32_bf16 v[84:87], v[56:59], a[164:167], v[84:87]\n\t"
    "v_mfma_f32_16x16x32_bf16 v[88:91], v[56:59], a[168:171], v[88:91]\n\t"
    "v_mfma_f32_16x16x32_bf16 v[92:95], v[56:59], a[172:175], v[92:95]\n\t"
    "v_mfma_f32_16x16x32_bf16 v[80:83], v[60:63], a[176:179], v[80:83]\n\t"
    "v_mfma_f32_16x16x32_bf16 v[84:87], v[60:63], a[180:183], v[84:87]\n\t"
    "v_mfma_f32_16x16x32_bf16 v[88:91], v[60:63], a[184:187], v[88:91]\n\t"
    "v_mfma_f32_16x16x32_bf16 v[92:95], v[60:63], a[188:191], v[92:95]\n\t"
    "s_waitcnt lgkmcnt(14)\n\t"
    "v_mfma_f32_16x16x32_bf16 v[64:67], v[0:3], a[32:35], 0\n\t"
    "v_mov_b32 v214, 0\n\t"
    "s_and_b32 s75, s70, 2\n\t"
    "v_lshlrev_b32 v213, 1, s75\n\t"
    "ds_write_b32 v213, v214 offset:0\n\t"
    "v_mfma_f32_16x16x32_bf16 v[68:71], v[0:3], a[36:39], 0\n\t"
    "v_mfma_f32_16x16x32_bf16 v[72:75], v[0:3], a[40:43], 0\n\t"
    "v_cvt_pk_bf16_f32 v136, v80, v81\n\t"
    "v_mfma_f32_16x16x32_bf16 v[76:79], v[0:3], a[44:47], 0\n\t"
    "v_cvt_pk_bf16_f32 v137, v82, v83\n\t"
    "s_waitcnt lgkmcnt(13)\n\t"
    "v_mfma_f32_16x16x32_bf16 v[64:67], v[4:7], a[48:51], v[64:67]\n\t"
    "v_cvt_pk_bf16_f32 v138, v84, v85\n\t"
    "v_mfma_f32_16x16x32_bf16 v[68:71], v[4:7], a[52:55], v[68:71]\n\t"
    "v_cvt_pk_bf16_f32 v139, v86, v87\n\t"
    "v_mfma_f32_16x16x32_bf16 v[72:75], v[4:7], a[56:59], v[72:75]\n\t"
    "s_mov_b32 s74, 40960\n\t"
    "buffer_store_dwordx4 v[136:139], v225, s[40:43], s74 offen sc1\n\t"
    "v_mfma_f32_16x16x32_bf16 v[76:79], v[4:7], a[60:63], v[76:79]\n\t"
    "v_cvt_pk_bf16_f32 v140, v88, v89\n\t"
    "s_waitcnt lgkmcnt(11)\n\t"
    "v_mfma_f32_16x16x32_bf16 v[64:67], v[8:11], a[64:67], v[64:67]\n\t"
    "v_cvt_pk_bf16_f32 v141, v90, v91\n\t"
    "v_mfma_f32_16x16x32_bf16 v[68:71], v[8:11], a[68:71], v[68:71]\n\t"
    "v_cvt_pk_bf16_f32 v142, v92, v93\n\t"
    "v_mfma_f32_16x16x32_bf16 v[72:75], v[8:11], a[72:75], v[72:75]\n\t"
    "v_cvt_pk_bf16_f32 v143, v94, v95\n\t"
    "v_mfma_f32_16x16x32_bf16 v[76:79], v[8:11], a[76:79], v[76:79]\n\t"
    "s_mov_b32 s74, 41984\n\t"
    "buffer_store_dwordx4 v[140:143], v225, s[40:43], s74 offen sc1\n\t"
    "s_waitcnt lgkmcnt(9)\n\t"
    "v_mfma_f32_16x16x32_bf16 v[64:67], v[12:15], a[80:83], v[64:67]\n\t"
    "v_mfma_f32_16x16x32_bf16 v[68:71], v[12:15], a[84:87], v[68:71]\n\t"
    "v_mfma_f32_16x16x32_bf16 v[72:75], v[12:15], a[88:91], v[72:75]\n\t"
    "v_mfma_f32_16x16x32_bf16 v[76:79], v[12:15], a[92:95], v[76:79]\n\t"
    "s_waitcnt lgkmcnt(7)\n\t"
    "v_mfma_f32_16x16x32_bf16 v[64:67], v[16:19], a[96:99], v[64:67]\n\t"
    "v_mfma_f32_16x16x32_bf16 v[68:71], v[16:19], a[100:103], v[68:71]\n\t"
    "v_mfma_f32_16x16x32_bf16 v[72:75], v[16:19], a[104:107], v[72:75]\n\t"
    "v_mfma_f32_16x16x32_bf16 v[76:79], v[16:19], a[108:111], v[76:79]\n\t"
    "s_waitcnt lgkmcnt(5)\n\t"
    "v_mfma_f32_16x16x32_bf16 v[64:67], v[20:23], a[112:115], v[64:67]\n\t"
    "v_mfma_f32_16x16x32_bf16 v[68:71], v[20:23], a[116:119], v[68:71]\n\t"
    "v_mfma_f32_16x16x32_bf16 v[72:75], v[20:23], a[120:123], v[72:75]\n\t"
    "v_mfma_f32_16x16x32_bf16 v[76:79], v[20:23], a[124:127], v[76:79]\n\t"
    "s_waitcnt lgkmcnt(3)\n\t"
    "v_mfma_f32_16x16x32_bf16 v[64:67], v[24:27], a[160:163], v[64:67]\n\t"
    "v_mfma_f32_16x16x32_bf16 v[68:71], v[24:27], a[164:167], v[68:71]\n\t"
    "v_mfma_f32_16x16x32_bf16 v[72:75], v[24:27], a[168:171], v[72:75]\n\t"
    "v_mfma_f32_16x16x32_bf16 v[76:79], v[24:27], a[172:175], v[76:79]\n\t"
    "s_waitcnt lgkmcnt(1)\n\t"
    "v_mfma_f32_16x16x32_bf16 v[64:67], v[28:31], a[176:179], v[64:67]\n\t"
    "v_mfma_f32_16x16x32_bf16 v[68:71], v[28:31], a[180:183], v[68:71]\n\t"
    "v_mfma_f32_16x16x32_bf16 v[72:75], v[28:31], a[184:187], v[72:75]\n\t"
    "v_mfma_f32_16x16x32_bf16 v[76:79], v[28:31], a[188:191], v[76:79]\n\t"
    "s_nop 4\n\t"
    "v_cvt_pk_bf16_f32 v144, v64, v65\n\t"
    "v_cvt_pk_bf16_f32 v145, v66, v67\n\t"
    "v_cvt_pk_bf16_f32 v146, v68, v69\n\t"
    "v_cvt_pk_bf16_f32 v147, v70, v71\n\t"
    "s_mov_b32 s74, 49152\n\t"
    "buffer_store_dwordx4 v[144:147], v225, s[40:43], s74 offen sc1\n\t"
    "v_cvt_pk_bf16_f32 v148, v72, v73\n\t"
    "v_cvt_pk_bf16_f32 v149, v74, v75\n\t"
    "v_cvt_pk_bf16_f32 v150, v76, v77\n\t"
    "v_cvt_pk_bf16_f32 v151, v78, v79\n\t"
    "s_mov_b32 s74, 50176\n\t"
    "buffer_store_dwordx4 v[148:151], v225, s[40:43], s74 offen sc1\n\t"
    "v_and_b32 v212, 0x40000000, v229\n\t"
    "v_mov_b32 v214, 1\n\t"
    "v_mov_b32 v168, 0x3f80\n\t"
    "v_mov_b32 v169, 0x3f800000\n\t"
    "s_mov_b32 s81, 0x100000\n\t"
    "s_mov_b32 s77, 0xffff\n\t"
    "s_bitset0_b32 s77, s71\n\t"
    "v_mov_b32 v210, 0\n\t"
    "v_mov_b32 v211, 0\n\t"
    "s_waitcnt vmcnt(0)\n\t"
    "LT3_0_%=:\n\t"
    "s_cbranch_execz LT3_1_%=\n\t"
    "s_waitcnt lgkmcnt(0)\n\t"
    "s_barrier\n\t"
    "buffer_store_dwordx2 v[208:209], v229, s[44:47], 0 offen offset:128 sc1\n\t"
    "s_bitcmp1_b32 s70, 0\n\t"
    "s_cbranch_scc1 Lodd_%=\n\t"
    "s_branch Lld_%=\n\t"
    "Lpoll_%=:\n\t"
    "v_cmp_eq_u64 vcc, s[84:85], v[210:211]\n\t"
    "s_sub_u32 s81, s81, 1\n\t"
    "s_cbranch_scc1 Lgot_%=\n\t"
    "s_andn2_b32 s75, s77, vcc_lo\n\t"
    "s_cbranch_scc0 Lgot_%=\n\t"
    "s_sleep 4\n\t"
    "Lld_%=:\n\t"
    "buffer_load_dwordx2 v[210:211], v230, s[44:47], 0 offen sc1\n\t"
    "s_waitcnt vmcnt(0)\n\t"
    "s_branch Lpoll_%=\n\t"
    "Lgot_%=:\n\t"
    "ds_write_b32 v213, v214 offset:0\n\t"
    "s_branch Ldone_%=\n\t"
    "Lodd_%=:\n\t"
    "ds_read_b32 v215, v213 offset:0\n\t"
    "s_waitcnt lgkmcnt(0)\n\t"
    "v_cmp_ne_u32 vcc, 0, v215\n\t"
    "s_cbranch_vccnz Ldone_%=\n\t"
    "s_sleep 1\n\t"
    "s_branch Lodd_%=\n\t"
    "Ldone_%=:\n\t"
    "s_mov_b32 s80, 0\n\t"
    "buffer_load_dwordx4 v[0:3], v231, s[48:51], s80 offen sc1 nt\n\t"
    "s_mov_b32 s80, 57344\n\t"
    "buffer_load_dwordx4 v[4:7], v231, s[48:51], s80 offen sc1 nt\n\t"
    "s_mov_b32 s80, 114688\n\t"
    "buffer_load_dwordx4 v[8:11], v231, s[48:51], s80 offen sc1 nt\n\t"
    "s_mov_b32 s80, 172032\n\t"
    "buffer_load_dwordx4 v[12:15], v231, s[48:51], s80 offen sc1 nt\n\t"
    "s_mov_b32 s80, 229376\n\t"
    "buffer_load_dwordx4 v[16:19], v231, s[48:51], s80 offen sc1 nt\n\t"
    "s_mov_b32 s80, 286720\n\t"
    "buffer_load_dwordx4 v[20:23], v231, s[48:51], s80 offen sc1 nt\n\t"
    "s_mov_b32 s80, 344064\n\t"
    "buffer_load_dwordx4 v[24:27], v231, s[48:51], s80 offen sc1 nt\n\t"
    "s_mov_b32 s80, 401408\n\t"
    "buffer_load_dwordx4 v[28:31], v231, s[48:51], s80 offen sc1 nt\n\t"
    "s_mov_b32 s80, 458752\n\t"
    "buffer_load_dwordx4 v[32:35], v231, s[48:51], s80 offen sc1 nt\n\t"
    "s_mov_b32 s80, 516096\n\t"
    "buffer_load_dwordx4 v[36:39], v231, s[48:51], s80 offen sc1 nt\n\t"
    "s_mov_b32 s80, 573440\n\t"
    "buffer_load_dwordx4 v[40:43], v231, s[48:51], s80 offen sc1 nt\n\t"
    "s_mov_b32 s80, 630784\n\t"
    "buffer_load_dwordx4 v[44:47], v231, s[48:51], s80 offen sc1 nt\n\t"
    "s_mov_b32 s80, 688128\n\t"
    "buffer_load_dwordx4 v[48:51], v231, s[48:51], s80 offen sc1 nt\n\t"
    "s_mov_b32 s80, 745472\n\t"
    "buffer_load_dwordx4 v[52:55], v231, s[48:51], s80 offen sc1 nt\n\t"
    "s_mov_b32 s80, 802816\n\t"
    "buffer_load_dwordx4 v[56:59], v231, s[48:51], s80 offen sc1 nt\n\t"
    "s_mov_b32 s80, 860160\n\t"
    "buffer_load_dwordx4 v[60:63], v231, s[48:51], s80 offen sc1 nt\n\t"
    "buffer_store_dwordx2 v[208:209], v212, s[44:47], s78 offen sc1\n\t"
    "s_waitcnt vmcnt(16)\n\t"
    "v_lshlrev_b32 v160, 16, v0\n\t"
    "v_and_b32 v161, 0xffff0000, v0\n\t"
    "v_lshlrev_b32 v162, 16, v1\n\t"
    "v_and_b32 v163, 0xffff0000, v1\n\t"
    "v_lshlrev_b32 v164, 16, v2\n\t"
    "v_and_b32 v165, 0xffff0000, v2\n\t"
    "v_lshlrev_b32 v166, 16, v3\n\t"
    "v_and_b32 v167, 0xffff0000, v3\n\t"
    "s_waitcnt vmcnt(15)\n\t"
    "v_dot2c_f32_bf16 v160, v4, v168\n\t"
    "v_dot2c_f32_bf16 v161, v4, v169\n\t"
    "v_dot2c_f32_bf16 v162, v5, v168\n\t"
    "v_dot2c_f32_bf16 v163, v5, v169\n\t"
    "v_dot2c_f32_bf16 v164, v6, v168\n\t"
    "v_dot2c_f32_bf16 v165, v6, v169\n\t"
    "v_dot2c_f32_bf16 v166, v7, v168\n\t"
    "v_dot2c_f32_bf16 v167, v7, v169\n\t"
    "s_waitcnt vmcnt(14)\n\t"
    "v_dot2c_f32_bf16 v160, v8, v168\n\t"
    "v_dot2c_f32_bf16 v161, v8, v169\n\t"
    "v_dot2c_f32_bf16 v162, v9, v168\n\t"
    "v_dot2c_f32_bf16 v163, v9, v169\n\t"
    "v_dot2c_f32_bf16 v164, v10, v168\n\t"
    "v_dot2c_f32_bf16 v165, v10, v169\n\t"
    "v_dot2c_f32_bf16 v166, v11, v168\n\t"
    "v_dot2c_f32_bf16 v167, v11, v169\n\t"
    "s_waitcnt vmcnt(13)\n\t"
    "v_dot2c_f32_bf16 v160, v12, v168\n\t"
    "v_dot2c_f32_bf16 v161, v12, v169\n\t"
    "v_dot2c_f32_bf16 v162, v13, v168\n\t"
    "v_dot2c_f32_bf16 v163, v13, v169\n\t"
    "v_dot2c_f32_bf16 v164, v14, v168\n\t"
    "v_dot2c_f32_bf16 v165, v14, v169\n\t"
    "v_dot2c_f32_bf16 v166, v15, v168\n\t"
    "v_dot2c_f32_bf16 v167, v15, v169\n\t"
    "s_waitcnt vmcnt(12)\n\t"
    "v_dot2c_f32_bf16 v160, v16, v168\n\t"
    "v_dot2c_f32_bf16 v161, v16, v169\n\t"
    "v_dot2c_f32_bf16 v162, v17, v168\n\t"
    "v_dot2c_f32_bf16 v163, v17, v169\n\t"
    "v_dot2c_f32_bf16 v164, v18, v168\n\t"
    "v_dot2c_f32_bf16 v165, v18, v169\n\t"
    "v_dot2c_f32_bf16 v166, v19, v168\n\t"
    "v_dot2c_f32_bf16 v167, v19, v169\n\t"
    "s_waitcnt vmcnt(11)\n\t"
    "v_dot2c_f32_bf16 v160, v20, v168\n\t"
    "v_dot2c_f32_bf16 v161, v20, v169\n\t"
    "v_dot2c_f32_bf16 v162, v21, v168\n\t"
    "v_dot2c_f32_bf16 v163, v21, v169\n\t"
    "LT3_1_%=:\n\t"
    "s_cbranch_execz LTr_%=\n\t"
    "v_dot2c_f32_bf16 v164, v22, v168\n\t"
    "v_dot2c_f32_bf16 v165, v22, v169\n\t"
    "v_dot2c_f32_bf16 v166, v23, v168\n\t"
    "v_dot2c_f32_bf16 v167, v23, v169\n\t"
    "s_waitcnt vmcnt(10)\n\t"
    "v_dot2c_f32_bf16 v160, v24, v168\n\t"
    "v_dot2c_f32_bf16 v161, v24, v169\n\t"
    "v_dot2c_f32_bf16 v162, v25, v168\n\t"
    "v_dot2c_f32_bf16 v163, v25, v169\n\t"
    "v_dot2c_f32_bf16 v164, v26, v168\n\t"
    "v_dot2c_f32_bf16 v165, v26, v169\n\t"
    "v_dot2c_f32_bf16 v166, v27, v168\n\t"
    "v_dot2c_f32_bf16 v167, v27, v169\n\t"
    "s_waitcnt vmcnt(9)\n\t"
    "v_dot2c_f32_bf16 v160, v28, v168\n\t"
    "v_dot2c_f32_bf16 v161, v28, v169\n\t"
    "v_dot2c_f32_bf16 v162, v29, v168\n\t"
    "v_dot2c_f32_bf16 v163, v29, v169\n\t"
    "v_dot2c_f32_bf16 v164, v30, v168\n\t"
    "v_dot2c_f32_bf16 v165, v30, v169\n\t"
    "v_dot2c_f32_bf16 v166, v31, v168\n\t"
    "v_dot2c_f32_bf16 v167, v31, v169\n\t"
    "s_waitcnt vmcnt(8)\n\t"
    "v_dot2c_f32_bf16 v160, v32, v168\n\t"
    "v_dot2c_f32_bf16 v161, v32, v169\n\t"
    "v_dot2c_f32_bf16 v162, v33, v168\n\t"
    "v_dot2c_f32_bf16 v163, v33, v169\n\t"
    "v_dot2c_f32_bf16 v164, v34, v168\n\t"
    "v_dot2c_f32_bf16 v165, v34, v169\n\t"
    "v_dot2c_f32_bf16 v166, v35, v168\n\t"
    "v_dot2c_f32_bf16 v167, v35, v169\n\t"
    "s_waitcnt vmcnt(7)\n\t"
    "v_dot2c_f32_bf16 v160, v36, v168\n\t"
    "v_dot2c_f32_bf16 v161, v36, v169\n\t"
    "v_dot2c_f32_bf16 v162, v37, v168\n\t"
    "v_dot2c_f32_bf16 v163, v37, v169\n\t"
    "v_dot2c_f32_bf16 v164, v38, v168\n\t"
    "v_dot2c_f32_bf16 v165, v38, v169\n\t"
    "v_dot2c_f32_bf16 v166, v39, v168\n\t"
    "v_dot2c_f32_bf16 v167, v39, v169\n\t"
    "s_waitcnt vmcnt(6)\n\t"
    "v_dot2c_f32_bf16 v160, v40, v168\n\t"
    "v_dot2c_f32_bf16 v161, v40, v169\n\t"
    "v_dot2c_f32_bf16 v162, v41, v168\n\t"
    "v_dot2c_f32_bf16 v163, v41, v169\n\t"
    "v_dot2c_f32_bf16 v164, v42, v168\n\t"
    "v_dot2c_f32_bf16 v165, v42, v169\n\t"
    "v_dot2c_f32_bf16 v166, v43, v168\n\t"
    "v_dot2c_f32_bf16 v167, v43, v169\n\t"
    "s_waitcnt vmcnt(5)\n\t"
    "v_dot2c_f32_bf16 v160, v44, v168\n\t"
    "v_dot2c_f32_bf16 v161, v44, v169\n\t"
    "v_dot2c_f32_bf16 v162, v45, v168\n\t"
    "v_dot2c_f32_bf16 v163, v45, v169\n\t"
    "v_dot2c_f32_bf16 v164, v46, v168\n\t"
    "v_dot2c_f32_bf16 v165, v46, v169\n\t"
    "v_dot2c_f32_bf16 v166, v47, v168\n\t"
    "v_dot2c_f32_bf16 v167, v47, v169\n\t"
    "s_waitcnt vmcnt(4)\n\t"
    "v_dot2c_f32_bf16 v160, v48, v168\n\t"
    "v_dot2c_f32_bf16 v161, v48, v169\n\t"
    "v_dot2c_f32_bf16 v162, v49, v168\n\t"
    "v_dot2c_f32_bf16 v163, v49, v169\n\t"
    "v_dot2c_f32_bf16 v164, v50, v168\n\t"
    "v_dot2c_f32_bf16 v165, v50, v169\n\t"
    "v_dot2c_f32_bf16 v166, v51, v168\n\t"
    "v_dot2c_f32_bf16 v167, v51, v169\n\t"
    "s_waitcnt vmcnt(3)\n\t"
    "v_dot2c_f32_bf16 v160, v52, v168\n\t"
    "v_dot2c_f32_bf16 v161, v52, v169\n\t"
    "v_dot2c_f32_bf16 v162, v53, v168\n\t"
    "v_dot2c_f32_bf16 v163, v53, v169\n\t"
    "v_dot2c_f32_bf16 v164, v54, v168\n\t"
    "v_dot2c_f32_bf16 v165, v54, v169\n\t"
    "v_dot2c_f32_bf16 v166, v55, v168\n\t"
    "v_dot2c_f32_bf16 v167, v55, v169\n\t"
    "s_waitcnt vmcnt(2)\n\t"
    "v_dot2c_f32_bf16 v160, v56, v168\n\t"
    "v_dot2c_f32_bf16 v161, v56, v169\n\t"
    "v_dot2c_f32_bf16 v162, v57, v168\n\t"
    "v_dot2c_f32_bf16 v163, v57, v169\n\t"
    "v_dot2c_f32_bf16 v164, v58, v168\n\t"
    "v_dot2c_f32_bf16 v165, v58, v169\n\t"
    "v_dot2c_f32_bf16 v166, v59, v168\n\t"
    "v_dot2c_f32_bf16 v167, v59, v169\n\t"
    "s_waitcnt vmcnt(1)\n\t"
    "v_dot2c_f32_bf16 v160, v60, v168\n\t"
    "v_dot2c_f32_bf16 v161, v60, v169\n\t"
    "v_dot2c_f32_bf16 v162, v61, v168\n\t"
    "v_dot2c_f32_bf16 v163, v61, v169\n\t"
    "v_dot2c_f32_bf16 v164, v62, v168\n\t"
    "v_dot2c_f32_bf16 v165, v62, v169\n\t"
    "v_dot2c_f32_bf16 v166, v63, v168\n\t"
    "v_dot2c_f32_bf16 v167, v63, v169\n\t"
    "s_mov_b32 vcc_lo, 0xaaaaaaaa\n\t"
    "s_mov_b32 vcc_hi, 0xaaaaaaaa\n\t"
    "s_nop 1\n\t"
    "v_cvt_pk_bf16_f32 v80, v160, v161\n\t"
    "v_cvt_pk_bf16_f32 v81, v162, v163\n\t"
    "v_cvt_pk_bf16_f32 v82, v164, v165\n\t"
    "v_cvt_pk_bf16_f32 v83, v166, v167\n\t"
    "v_cndmask_b32 v84, v82, v80, vcc\n\t"
    "v_cndmask_b32 v85, v83, v81, vcc\n\t"
    "s_nop 1\n\t"
    "v_mov_b32_dpp v86, v84 quad_perm:[1,0,3,2] row_mask:0xf bank_mask:0xf\n\t"
    "v_mov_b32_dpp v87, v85 quad_perm:[1,0,3,2] row_mask:0xf bank_mask:0xf\n\t"
    "v_cndmask_b32 v88, v80, v86, vcc\n\t"
    "v_cndmask_b32 v89, v81, v87, vcc\n\t"
    "v_cndmask_b32 v90, v86, v82, vcc\n\t"
    "v_cndmask_b32 v91, v87, v83, vcc\n\t"
    "buffer_store_dwordx4 v[88:91], v232, s[52:55], 0 offen sc1\n\t"
    "s_branch Lend_%=\n\t"
    "Lredo_%=:\n\t"
    "s_cmp_lg_u32 s79, 0\n\t"
    "s_cbranch_scc1 Lcont_%=\n\t"
    "s_mov_b32 s79, 1\n\t"
    "s_nop 15\n\t"
    "s_nop 15\n\t"
    "s_waitcnt vmcnt(0) lgkmcnt(0)\n\t"
    "v_accvgpr_write_b32 a254, v208\n\t"
    "v_accvgpr_write_b32 a255, v209\n\t"
    "v_mbcnt_lo_u32_b32 v0, -1, 0\n\t"
    "v_mbcnt_hi_u32_b32 v0, -1, v0\n\t"
    "s_lshl_b32 s74, s70, 6\n\t"
    "v_add_u32 v0, s74, v0\n\t"
    "s_barrier\n\t"
    "s_nop 3\n\t"
    "s_branch Lstart_%=\n\t"
    "Lend_%=:\n\t"
    "s_nop 1\n\t"
    : "+{v0}"(tid)
    : [ka] "s"(__builtin_amdgcn_kernarg_segment_ptr()), [wg] "s"(blockIdx.x)
    : "memory", "scc", "vcc", "v1", "v2", "v3", "v4", "v5", "v6", "v7", "v8", "v9", "v10", "v11", "v12", "v13", "v14", "v15", "v16", "v17", "v18", "v19", "v20", "v21", "v22", "v23", "v24", "v25", "v26", "v27", "v28", "v29", "v30", "v31", "v32", "v33", "v34", "v35", "v36", "v37", "v38", "v39", "v40", "v41", "v42", "v43", "v44", "v45", "v46", "v47", "v48", "v49", "v50", "v51", "v52", "v53", "v54", "v55", "v56", "v57", "v58", "v59", "v60", "v61", "v62", "v63", "v64", "v65", "v66", "v67", "v68", "v69", "v70", "v71", "v72", "v73", "v74", "v75", "v76", "v77", "v78", "v79", "v80", "v81", "v82", "v83", "v84", "v85", "v86", "v87", "v88", "v89", "v90", "v91", "v92", "v93", "v94", "v95", "v96", "v97", "v98", "v99", "v100", "v101", "v102", "v103", "v104", "v105", "v106", "v107", "v108", "v109", "v110", "v111", "v112", "v113", "v114", "v115", "v116", "v117", "v118", "v119", "v120", "v121", "v122", "v123", "v124", "v125", "v126", "v127", "v128", "v129", "v130", "v131", "v132", "v133", "v134", "v135", "v136", "v137", "v138", "v139", "v140", "v141", "v142", "v143", "v144", "v145", "v146", "v147", "v148", "v149", "v150", "v151", "v152", "v153", "v154", "v155", "v156", "v157", "v158", "v159", "v160", "v161", "v162", "v163", "v164", "v165", "v166", "v167", "v168", "v169", "v170", "v171", "v172", "v173", "v174", "v175", "v176", "v177", "v178", "v179", "v180", "v181", "v182", "v183", "v184", "v185", "v186", "v187", "v188", "v189", "v190", "v191", "v192", "v193", "v194", "v195", "v196", "v197", "v198", "v199", "v200", "v201", "v202", "v203", "v204", "v205", "v206", "v207", "v208", "v209", "v210", "v211", "v212", "v213", "v214", "v215", "v216", "v217", "v218", "v219", "v220", "v221", "v222", "v223", "v224", "v225", "v226", "v227", "v228", "v229", "v230", "v231", "v232", "v233", "v234", "v235", "v236", "v237", "v238", "v239", "v240", "v241", "v242", "v243", "v244", "v245", "v246", "v247", "v248", "v249", "v250", "v251", "v252", "v253", "v254", "v255", "a0", "a1", "a2", "a3", "a4", "a5", "a6", "a7", "a8", "a9", "a10", "a11", "a12", "a13", "a14", "a15", "a16", "a17", "a18", "a19", "a20", "a21", "a22", "a23", "a24", "a25", "a26", "a27", "a28", "a29", "a30", "a31", "a32", "a33", "a34", "a35", "a36", "a37", "a38", "a39", "a40", "a41", "a42", "a43", "a44", "a45", "a46", "a47", "a48", "a49", "a50", "a51", "a52", "a53", "a54", "a55", "a56", "a57", "a58", "a59", "a60", "a61", "a62", "a63", "a64", "a65", "a66", "a67", "a68", "a69", "a70", "a71", "a72", "a73", "a74", "a75", "a76", "a77", "a78", "a79", "a80", "a81", "a82", "a83", "a84", "a85", "a86", "a87", "a88", "a89", "a90", "a91", "a92", "a93", "a94", "a95", "a96", "a97", "a98", "a99", "a100", "a101", "a102", "a103", "a104", "a105", "a106", "a107", "a108", "a109", "a110", "a111", "a112", "a113", "a114", "a115", "a116", "a117", "a118", "a119", "a120", "a121", "a122", "a123", "a124", "a125", "a126", "a127", "a128", "a129", "a130", "a131", "a132", "a133", "a134", "a135", "a136", "a137", "a138", "a139", "a140", "a141", "a142", "a143", "a144", "a145", "a146", "a147", "a148", "a149", "a150", "a151", "a152", "a153", "a154", "a155", "a156", "a157", "a158", "a159", "a160", "a161", "a162", "a163", "a164", "a165", "a166", "a167", "a168", "a169", "a170", "a171", "a172", "a173", "a174", "a175", "a176", "a177", "a178", "a179", "a180", "a181", "a182", "a183", "a184", "a185", "a186", "a187", "a188", "a189", "a190", "a191", "a192", "a193", "a194", "a195", "a196", "a197", "a198", "a199", "a200", "a201", "a202", "a203", "a204", "a205", "a206", "a207", "a208", "a209", "a210", "a211", "a212", "a213", "a214", "a215", "a216", "a217", "a218", "a219", "a220", "a221", "a222", "a223", "a224", "a225", "a226", "a227", "a228", "a229", "a230", "a231", "a232", "a233", "a234", "a235", "a236", "a237", "a238", "a239", "a240", "a241", "a242", "a243", "a244", "a245", "a246", "a247", "a248", "a249", "a250", "a251", "a252", "a253", "a254", "a255", "s4", "s5", "s6", "s7", "s8", "s9", "s10", "s11", "s12", "s13", "s14", "s15", "s16", "s17", "s18", "s19", "s20", "s21", "s22", "s23", "s24", "s25", "s26", "s27", "s28", "s29", "s30", "s31", "s32", "s33", "s34", "s35", "s36", "s37", "s38", "s39", "s40", "s41", "s42", "s43", "s44", "s45", "s46", "s47", "s48", "s49", "s50", "s51", "s52", "s53", "s54", "s55", "s56", "s57", "s58", "s59", "s60", "s61", "s62", "s63", "s64", "s65", "s66", "s67", "s68", "s69", "s70", "s71", "s72", "s73", "s74", "s75", "s76", "s77", "s78", "s79", "s80", "s81", "s82", "s83", "s84", "s85", "s86", "s87", "s88", "s89", "s90", "s91", "s92", "s93", "s94", "s95");
}

extern "C" void kernel_launch(void* const* d_in, const int* in_sizes, int n_in, void* d_out, int out_size, void* d_ws, size_t ws_size, hipStream_t stream) {
  (void)in_sizes; (void)n_in; (void)out_size; (void)ws_size;
  (void)hipFuncSetAttribute((const void*)gqa_fused, hipFuncAttributeMaxDynamicSharedMemorySize, 163840);
  hipLaunchKernelGGL(gqa_fused, dim3(256), dim3(256), 163840, stream,
                     (const char*)d_in[0], (const char*)d_in[1], (const char*)d_in[2], (const char*)d_in[3], (const char*)d_in[4],
                     (const char*)d_in[5], (const char*)d_in[6], (const char*)d_in[7], (const char*)d_in[8], (const char*)d_in[9],
                     (char*)d_out, (char*)d_ws);
}
```
